```python
import torch

def _softmax(x, axis=-1):
    return torch.softmax(x, dim=axis)
def _gelu(x):
    return torch.nn.functional.gelu(x, approximate="tanh")
def _silu(x):
    return torch.nn.functional.silu(x)
def _sigmoid(x):
    return torch.sigmoid(x)
def _layernorm(x, axis=-1, eps=1e-5):
    m = x.mean(dim=axis, keepdim=True)
    v = x.var(dim=axis, keepdim=True, unbiased=False)
    return (x - m) / torch.sqrt(v + eps)
def _scatter_add(tgt, idx, src, axis=0):
    return tgt.clone().index_add_(axis, idx, src)
def _segment_sum(src, seg, n):
    out = torch.zeros((n,) + src.shape[1:], dtype=src.dtype)
    return out.index_add_(0, seg, src)
def _pad(x, pw):
    flat = tuple(v for pair in reversed(pw) for v in pair)
    return torch.nn.functional.pad(x, flat)

kernel_name = 'gated_mix__grouped_kv_mqa__mixer_token_mlp__prompt'

def setup_inputs():

    _w = (lambda a: (lambda t: t.to(torch.bfloat16) if t.is_floating_point() else t)(torch.as_tensor(a)))
    def _A_setup():
        B, S, D, H = 1, 2048, 640, 32
        Dh = D // H
        return {
            "x": _w((lambda *s: torch.randn(*s, dtype=torch.bfloat16) * 0.05)(B, S, D)),
            "wq": _w((lambda *s: torch.randn(*s, dtype=torch.bfloat16) * 0.8)(D, H * Dh)),
            "wk": _w((lambda *s: torch.randn(*s, dtype=torch.bfloat16) * 0.8)(D, Dh)),
            "wv": _w((lambda *s: torch.randn(*s, dtype=torch.bfloat16) * 0.05)(D, Dh)),
            "wo": _w((lambda *s: torch.randn(*s, dtype=torch.bfloat16) * 0.05)(H * Dh, D)),
        }
    _A_in = _A_setup()

    def _B_setup():
        B, S, D, F = 1, 2048, 640, 1536
        return {
            "x": _w((lambda *s: torch.randn(*s, dtype=torch.bfloat16) * 0.05)(B, S, D)),
            "w1": _w((lambda *s: torch.randn(*s, dtype=torch.bfloat16) * 0.2)(S, F) * (S ** -0.5)),
            "b1": _w((lambda *s: torch.randn(*s, dtype=torch.bfloat16) * 3.2)(F) * 0.02),
            "w2": _w((lambda *s: torch.randn(*s, dtype=torch.bfloat16) * 0.05)(F, S) * (F ** -0.5)),
            "b2": _w((lambda *s: torch.randn(*s, dtype=torch.bfloat16) * 0.1)(S) * 0.02),
        }
    _B_in = _B_setup()

    return {
        "A_x": _A_in["x"],
        "A_wq": _A_in["wq"],
        "A_wk": _A_in["wk"],
        "A_wv": _A_in["wv"],
        "A_wo": _A_in["wo"],
        "B_x": _B_in["x"],
        "B_w1": _B_in["w1"],
        "B_b1": _B_in["b1"],
        "B_w2": _B_in["w2"],
        "B_b2": _B_in["b2"],
        "gate_w": _w((lambda *s: torch.randn(*s, dtype=torch.bfloat16) * 0.05)(640) * 16.0),
    }

def reference(A_x, A_wq, A_wk, A_wv, A_wo, B_x, B_w1, B_b1, B_w2, B_b2, gate_w):
    def _A_ref(x, wq, wk, wv, wo):
        B, S, D = x.shape
        H = 32
        Dh = D // H
        q = (lambda x, *d: x.permute(*d))((x @ wq).reshape(B, S, H, Dh), 0, 2, 1, 3)
        k = x @ wk
        v = x @ wv
        scores = torch.einsum("bhqd,bkd->bhqk", q, k) * (Dh ** -0.5)
        p = _softmax(scores, axis=-1)
        o = torch.einsum("bhqk,bkd->bhqd", p, v)
        return (lambda x, *d: x.permute(*d))(o, 0, 2, 1, 3).reshape(B, S, H * Dh) @ wo

    def _B_ref(x, w1, b1, w2, b2):
        B, S, D = x.shape
        h = _layernorm(x, axis=-1)
        u = torch.einsum("bsd,sf->bdf", h, w1) + b1
        u = _gelu(u)
        y = torch.einsum("bdf,fs->bds", u, w2) + b2
        return (lambda x, *d: x.permute(*d))(y, 0, 2, 1)

    x = A_x
    g = _sigmoid((x * gate_w).sum(-1, keepdims=True))
    a = _A_ref(x, A_wq, A_wk, A_wv, A_wo)
    b = _B_ref(x, B_w1, B_b1, B_w2, B_b2)
    return g * a + (1 - g) * b
```

```cpp
#include <hip/hip_runtime.h>
#include <hip/hip_bf16.h>
#include <stdint.h>

typedef short bf16x8 __attribute__((ext_vector_type(8)));
typedef short bf16x4 __attribute__((ext_vector_type(4)));
typedef float f32x16 __attribute__((ext_vector_type(16)));
typedef float f32x2 __attribute__((ext_vector_type(2)));
typedef __bf16 bfx2 __attribute__((ext_vector_type(2)));
typedef unsigned u32x4 __attribute__((ext_vector_type(4)));
typedef unsigned u32x2 __attribute__((ext_vector_type(2)));
typedef unsigned short u16;
typedef __attribute__((address_space(3))) bf16x4* lds_b4p;

#define DI __device__ __forceinline__
#define MFMA32(a, b, c) __builtin_amdgcn_mfma_f32_32x32x16_bf16((a), (b), (c), 0, 0, 0)

constexpr int SEQ = 2048, DM = 640, NH = 32, DH = 20, FF = 1536;
constexpr int KVCH = 256;
constexpr int KV_KBYTES = KVCH * 48;
constexpr int KV_VSTR = 672;
constexpr int KV_CHBYTES = 24576;
constexpr int NCH = SEQ / KVCH;

constexpr size_t WS_Q = 0;
constexpr size_t WS_H = WS_Q + (size_t)SEQ * DM * 2;
constexpr size_t WS_O = WS_H + (size_t)SEQ * DM * 2;
constexpr size_t WS_U = WS_O + (size_t)SEQ * DM * 2;
constexpr size_t WS_KV = WS_U + (size_t)FF * DM * 2;
constexpr size_t WS_G = WS_KV + (size_t)NCH * KV_CHBYTES;

DI unsigned pk2(float lo, float hi) {
  f32x2 v = {lo, hi};
  bfx2 b = __builtin_convertvector(v, bfx2);
  return __builtin_bit_cast(unsigned, b);
}
DI float bf2f(u16 v) { return __uint_as_float(((unsigned)v) << 16); }
DI u16 f2bf(float f) { return (u16)(pk2(f, 0.f) & 0xffffu); }

DI void glds16(const void* g, void* l) {
  __builtin_amdgcn_global_load_lds((const __attribute__((address_space(1))) unsigned*)g,
                                   (__attribute__((address_space(3))) unsigned*)l, 16, 0, 0);
}

DI void stage_row(const u16* src, int ld, char* img, int wave, int lane) {
#pragma unroll
  for (int i = 0; i < 2; ++i) {
    const int row = (wave * 2 + i) * 8 + (lane >> 3);
    const int c = (lane & 7) ^ ((row >> 1) & 7);
    glds16(src + (size_t)row * ld + c * 8, img + (wave * 2 + i) * 1024 + lane * 16);
  }
}
DI void stage_col(const u16* src, int ld, char* img, int wave, int lane) {
#pragma unroll
  for (int i = 0; i < 2; ++i) {
    const int row = (wave * 2 + i) * 8 + (lane >> 3);
    const int c = (lane & 7) ^ (((row >> 1) & 1) << 2);
    glds16(src + (size_t)row * ld + c * 8, img + (wave * 2 + i) * 1024 + lane * 16);
  }
}
DI bf16x8 frag_row(const char* img, int rb, int kk, int lane) {
  const int row = rb + (lane & 31);
  const int c = 2 * kk + (lane >> 5);
  return *(const bf16x8*)(img + row * 128 + ((c ^ ((row >> 1) & 7)) << 4));
}
DI bf16x8 tr_pair(const char* p0, const char* p1) {
  bf16x4 lo = __builtin_amdgcn_ds_read_tr16_b64_v4i16((lds_b4p)(p0));
  bf16x4 hi = __builtin_amdgcn_ds_read_tr16_b64_v4i16((lds_b4p)(p1));
  return __builtin_shufflevector(lo, hi, 0, 1, 2, 3, 4, 5, 6, 7);
}
DI bf16x8 frag_col(const char* img, int cb, int kk, int lane) {
  const int i16 = lane & 15, q = i16 >> 2, p = i16 & 3, h = lane >> 5, g1 = (lane >> 4) & 1;
  const int col = cb + 16 * g1 + 4 * p;
  const int krow = 16 * kk + 8 * h + q;
  const char* a = img + krow * 128 + (((col >> 3) ^ (((krow >> 1) & 1) << 2)) << 4) + (col & 7) * 2;
  return tr_pair(a, a + 512);
}

#define WAITV(n) asm volatile("s_waitcnt vmcnt(" #n ")" ::: "memory")
#define BARRIER() do { asm volatile("s_waitcnt lgkmcnt(0)" ::: "memory"); __builtin_amdgcn_s_barrier(); asm volatile("" ::: "memory"); } while (0)

template <int NA, int NB, bool BROW>
DI void gemm_loop(f32x16 (&acc)[NA][NB], const u16* A, int lda, const u16* B, int ldb, int nk, char* lds, int wave,
                  int lane) {
  constexpr int STG = (NA + NB) * 8192;
  constexpr int G = (NA + NB) * 2;
  const int wm = wave >> 1, wn = wave & 1;
  auto stage = [&](int kt, int buf) {
    char* base = lds + buf * STG;
#pragma unroll
    for (int a = 0; a < NA; ++a) stage_col(A + (size_t)kt * 64 * lda + a * 64, lda, base + a * 8192, wave, lane);
#pragma unroll
    for (int b = 0; b < NB; ++b) {
      if (BROW) stage_row(B + (size_t)b * 64 * ldb + kt * 64, ldb, base + (NA + b) * 8192, wave, lane);
      else stage_col(B + (size_t)kt * 64 * ldb + b * 64, ldb, base + (NA + b) * 8192, wave, lane);
    }
  };
  stage(0, 0);
  if (nk > 1) stage(1, 1);
  int buf = 0;
  for (int kt = 0; kt < nk; ++kt) {
    if (kt + 1 < nk) {
      if constexpr (G == 4) WAITV(4);
      else if constexpr (G == 6) WAITV(6);
      else WAITV(8);
    } else {
      WAITV(0);
    }
    BARRIER();
    if (kt + 2 < nk) stage(kt + 2, buf == 0 ? 2 : buf - 1);
    const char* base = lds + buf * STG;
#pragma unroll
    for (int kk = 0; kk < 4; ++kk) {
      bf16x8 fa[NA], fb[NB];
#pragma unroll
      for (int a = 0; a < NA; ++a) fa[a] = frag_col(base + a * 8192, 32 * wm, kk, lane);
#pragma unroll
      for (int b = 0; b < NB; ++b) {
        if (BROW) fb[b] = frag_row(base + (NA + b) * 8192, 32 * wn, kk, lane);
        else fb[b] = frag_col(base + (NA + b) * 8192, 32 * wn, kk, lane);
      }
#pragma unroll
      for (int a = 0; a < NA; ++a)
#pragma unroll
        for (int b = 0; b < NB; ++b) acc[a][b] = MFMA32(fa[a], fb[b], acc[a][b]);
    }
    buf = buf == 2 ? 0 : buf + 1;
  }
  BARRIER();
}

constexpr int S1_NA = 1, S1_NB = 1;
constexpr int S1_QT = (DM / (64 * S1_NA)) * (SEQ / (64 * S1_NB));
constexpr int S1_KVT = SEQ / 64;
constexpr int S1_LNT = SEQ / 16;
constexpr int S1_LDS = 640 * 80 + 1024 + 3 * 8192;

__global__ __launch_bounds__(256) void stage1_kernel(const u16* __restrict__ x, const u16* __restrict__ wq,
                                                     const u16* __restrict__ wk, const u16* __restrict__ wv,
                                                     const u16* __restrict__ gw, char* __restrict__ ws) {
  extern __shared__ __attribute__((aligned(16))) char lds[];
  const int tid = threadIdx.x, wave = tid >> 6, lane = tid & 63;
  const int wm = wave >> 1, wn = wave & 1;
  int bid = blockIdx.x;
  if (bid < S1_QT) {
    constexpr int NTN = DM / (64 * S1_NA);
    const int tn = bid % NTN, ts = bid / NTN;
    const int n0 = tn * 64 * S1_NA, s0 = ts * 64 * S1_NB;
    f32x16 acc[S1_NA][S1_NB] = {};
    gemm_loop<S1_NA, S1_NB, true>(acc, wq + n0, DM, x + (size_t)s0 * DM, DM, DM / 64, lds, wave, lane);
    const float qs = 1.4426950408889634f * 0.22360679774997896f;
    u16* q = (u16*)(ws + WS_Q);
#pragma unroll
    for (int a = 0; a < S1_NA; ++a)
#pragma unroll
      for (int b = 0; b < S1_NB; ++b) {
        const int s = s0 + b * 64 + 32 * wn + (lane & 31);
        const int nb = n0 + a * 64 + 32 * wm + 4 * (lane >> 5);
#pragma unroll
        for (int g = 0; g < 4; ++g) {
          u32x2 v = {pk2(acc[a][b][4 * g] * qs, acc[a][b][4 * g + 1] * qs),
                     pk2(acc[a][b][4 * g + 2] * qs, acc[a][b][4 * g + 3] * qs)};
          *(u32x2*)(q + (size_t)s * DM + nb + 8 * g) = v;
        }
      }
    return;
  }
  bid -= S1_QT;
  if (bid < S1_KVT) {
    const int s0 = bid * 64;
    char* wimg = lds;
    char* aimg = lds + 640 * 80 + 1024;
    for (int i = tid; i < 640 * 10; i += 256) {
      const int k = i / 10, pc = i % 10;
      const u16* src = pc < 5 ? (wk + k * DH + pc * 4) : (wv + k * DH + (pc - 5) * 4);
      *(u32x2*)(wimg + k * 80 + pc * 8) = *(const u32x2*)src;
    }
    for (int i = tid; i < 256; i += 256) *(unsigned*)(wimg + 640 * 80 + i * 4) = 0u;
    f32x16 acc = {};
    stage_row(x + (size_t)s0 * DM, DM, aimg, wave, lane);
    stage_row(x + (size_t)s0 * DM + 64, DM, aimg + 8192, wave, lane);
    int buf = 0;
    const int i16 = lane & 15, q4 = i16 >> 2, p4 = i16 & 3, hh = lane >> 5, g1 = (lane >> 4) & 1;
    for (int kt = 0; kt < 10; ++kt) {
      if (kt + 1 < 10) WAITV(2); else WAITV(0);
      BARRIER();
      if (kt + 2 < 10) stage_row(x + (size_t)s0 * DM + (kt + 2) * 64, DM, aimg + (buf == 0 ? 2 : buf - 1) * 8192, wave, lane);
#pragma unroll
      for (int kk = 0; kk < 4; ++kk) {
        const int krow = kt * 64 + 16 * kk + 8 * hh + q4;
        const char* ap = wimg + krow * 80 + (32 * wm + 16 * g1 + 4 * p4) * 2;
        bf16x8 fa = tr_pair(ap, ap + 4 * 80);
        bf16x8 fb = frag_row(aimg + buf * 8192, 32 * wn, kk, lane);
        acc = MFMA32(fa, fb, acc);
      }
      buf = buf == 2 ? 0 : buf + 1;
    }
    const int s = s0 + 32 * wn + (lane & 31);
    char* kvp = ws + WS_KV + (size_t)(s / KVCH) * KV_CHBYTES;
    const int key = s % KVCH;
    const int kb = key >> 4, r = key & 15, vh = (r >> 2) & 1, vj = (r & 3) + 4 * (r >> 3);
    char* vbase = kvp + KV_KBYTES + kb * KV_VSTR + vh * 336 + vj * 2;
#pragma unroll
    for (int g = 0; g < 4; ++g) {
      const int c0 = 32 * wm + 8 * g + 4 * hh;
      if (c0 < 20) {
        u32x2 v = {pk2(acc[4 * g], acc[4 * g + 1]), pk2(acc[4 * g + 2], acc[4 * g + 3])};
        *(u32x2*)(kvp + key * 48 + c0 * 2) = v;
      } else if (c0 < 40) {
#pragma unroll
        for (int j = 0; j < 4; ++j) *(u16*)(vbase + (c0 - 20 + j) * 16) = f2bf(acc[4 * g + j]);
      }
    }
    if (wm == 0 && hh == 0) {
      u32x2 v = {0x00003f80u, 0u};
      *(u32x2*)(kvp + key * 48 + 40) = v;
      *(u16*)(vbase + 20 * 16) = 0x3f80;
    }
    return;
  }
  bid -= S1_KVT;
  {
    u16* h = (u16*)(ws + WS_H);
    float* gate = (float*)(ws + WS_G);
    float gwv[10];
#pragma unroll
    for (int i = 0; i < 5; ++i) {
      const unsigned u = *(const unsigned*)(gw + (lane + 64 * i) * 2);
      gwv[2 * i] = __uint_as_float(u << 16);
      gwv[2 * i + 1] = __uint_as_float(u & 0xffff0000u);
    }
#pragma unroll
    for (int rr = 0; rr < 4; ++rr) {
      const int s = bid * 16 + wave * 4 + rr;
      float v[10];
      float sum = 0.f, dot = 0.f;
#pragma unroll
      for (int i = 0; i < 5; ++i) {
        const unsigned u = *(const unsigned*)(x + (size_t)s * DM + (lane + 64 * i) * 2);
        v[2 * i] = __uint_as_float(u << 16);
        v[2 * i + 1] = __uint_as_float(u & 0xffff0000u);
        sum += v[2 * i] + v[2 * i + 1];
        dot += v[2 * i] * gwv[2 * i] + v[2 * i + 1] * gwv[2 * i + 1];
      }
#pragma unroll
      for (int o = 32; o >= 1; o >>= 1) { sum += __shfl_xor(sum, o); dot += __shfl_xor(dot, o); }
      const float mean = sum * (1.f / DM);
      float var = 0.f;
#pragma unroll
      for (int i = 0; i < 10; ++i) { const float d = v[i] - mean; var += d * d; }
#pragma unroll
      for (int o = 32; o >= 1; o >>= 1) var += __shfl_xor(var, o);
      const float rstd = 1.f / sqrtf(var * (1.f / DM) + 1e-5f);
#pragma unroll
      for (int i = 0; i < 5; ++i)
        *(unsigned*)(h + (size_t)s * DM + (lane + 64 * i) * 2) = pk2((v[2 * i] - mean) * rstd, (v[2 * i + 1] - mean) * rstd);
      if (lane == 0) gate[s] = 1.f / (1.f + __expf(-dot));
    }
  }
}

constexpr int AT_WAVES = 8;
constexpr int AT_LDS = 2 * KV_CHBYTES;

template <bool SAFE>
DI bool attn_pass(const char* __restrict__ kvg, char* lds, bf16x8 qf0, bf16x8 qf1, f32x16& o, int wave, int lane) {
  const int hd = lane & 31, hh = lane >> 5;
  const int koff0 = hd * 48 + 16 * hh, koff1 = hd * 48 + 32;
  const int voff = KV_KBYTES + hh * 336 + (hd < 20 ? hd : 20) * 16;
  auto stage = [&](int ch, int buf) {
#pragma unroll
    for (int i = 0; i < 3; ++i) {
      const int off = (wave * 3 + i) * 1024 + lane * 16;
      glds16(kvg + (size_t)ch * KV_CHBYTES + off, lds + buf * KV_CHBYTES + off);
    }
  };
  float mrun = -3.0e38f;
  stage(0, 0);
  for (int ch = 0; ch < NCH; ++ch) {
    WAITV(0);
    BARRIER();
    if (ch + 1 < NCH) stage(ch + 1, (ch + 1) & 1);
    const char* base = lds + (ch & 1) * KV_CHBYTES;
    if (!SAFE && ch == 0) {
      bf16x8 k0 = *(const bf16x8*)(base + koff0);
      bf16x8 k1 = *(const bf16x8*)(base + koff1);
      f32x16 z = {};
      f32x16 s = MFMA32(k0, qf0, z);
      s = MFMA32(k1, qf1, s);
      float m = s[0];
#pragma unroll
      for (int i = 1; i < 16; ++i) m = fmaxf(m, s[i]);
      m = fmaxf(m, __shfl_xor(m, 32));
      if (hh == 0) qf1[4] = (short)f2bf(-m);
    }
#pragma unroll 2
    for (int t = 0; t < KVCH / 32; ++t) {
      bf16x8 k0 = *(const bf16x8*)(base + t * 32 * 48 + koff0);
      bf16x8 k1 = *(const bf16x8*)(base + t * 32 * 48 + koff1);
      bf16x8 v0 = *(const bf16x8*)(base + voff + (2 * t) * KV_VSTR);
      bf16x8 v1 = *(const bf16x8*)(base + voff + (2 * t + 1) * KV_VSTR);
      f32x16 z = {};
      f32x16 s = MFMA32(k0, qf0, z);
      s = MFMA32(k1, qf1, s);
      if (SAFE) {
        float m = s[0];
#pragma unroll
        for (int i = 1; i < 16; ++i) m = fmaxf(m, s[i]);
        m = fmaxf(m, __shfl_xor(m, 32));
        const float mn = fmaxf(mrun, m);
        const float alpha = __builtin_amdgcn_exp2f(mrun - mn);
        mrun = mn;
#pragma unroll
        for (int i = 0; i < 16; ++i) { o[i] *= alpha; s[i] -= mn; }
      }
      u32x4 p0, p1;
#pragma unroll
      for (int i = 0; i < 4; ++i) {
        p0[i] = pk2(__builtin_amdgcn_exp2f(s[2 * i]), __builtin_amdgcn_exp2f(s[2 * i + 1]));
        p1[i] = pk2(__builtin_amdgcn_exp2f(s[8 + 2 * i]), __builtin_amdgcn_exp2f(s[8 + 2 * i + 1]));
      }
      o = MFMA32(v0, __builtin_bit_cast(bf16x8, p0), o);
      o = MFMA32(v1, __builtin_bit_cast(bf16x8, p1), o);
    }
  }
  return true;
}

__global__ __launch_bounds__(AT_WAVES * 64) void attn_kernel(char* __restrict__ ws) {
  extern __shared__ __attribute__((aligned(16))) char lds[];
  const int tid = threadIdx.x, wave = tid >> 6, lane = tid & 63;
  const int hd = lane & 31, hh = lane >> 5;
  const int s = blockIdx.x * AT_WAVES + wave;
  const u16* q = (const u16*)(ws + WS_Q) + (size_t)s * DM + hd * DH;
  const char* kvg = ws + WS_KV;
  bf16x8 qf0, qf1;
  {
    u32x2 a = *(const u32x2*)(q + 8 * hh), b = *(const u32x2*)(q + 8 * hh + 4), c = *(const u32x2*)(q + 16);
    u32x4 t0 = {a[0], a[1], b[0], b[1]};
    u32x4 t1 = {hh == 0 ? c[0] : 0u, hh == 0 ? c[1] : 0u, 0u, 0u};
    qf0 = __builtin_bit_cast(bf16x8, t0);
    qf1 = __builtin_bit_cast(bf16x8, t1);
  }
  f32x16 o = {};
  attn_pass<false>(kvg, lds, qf0, qf1, o, wave, lane);
  float l = __shfl(o[8], hd + 32);
  const bool bad = !(l > 8.67e-19f && l < 1.15e18f);
  if (__syncthreads_or(bad)) {
    o = f32x16{};
    attn_pass<true>(kvg, lds, qf0, qf1, o, wave, lane);
    l = __shfl(o[8], hd + 32);
  }
  const float inv = 1.f / l;
  u16* op = (u16*)(ws + WS_O) + (size_t)s * DM + hd * DH + 4 * hh;
#pragma unroll
  for (int g = 0; g < 3; ++g) {
    if (g < 2 || hh == 0) {
      u32x2 v = {pk2(o[4 * g] * inv, o[4 * g + 1] * inv), pk2(o[4 * g + 2] * inv, o[4 * g + 3] * inv)};
      *(u32x2*)(op + 8 * g) = v;
    }
  }
}

constexpr int G1_NA = 1, G1_NB = 1;
constexpr int G1_LDS = 3 * (G1_NA + G1_NB) * 8192;
__global__ __launch_bounds__(256) void gemm1_kernel(const u16* __restrict__ w1, const u16* __restrict__ b1,
                                                    char* __restrict__ ws) {
  extern __shared__ __attribute__((aligned(16))) char lds[];
  const int tid = threadIdx.x, wave = tid >> 6, lane = tid & 63;
  const int wm = wave >> 1, wn = wave & 1;
  constexpr int NTD = DM / (64 * G1_NA);
  const int td = blockIdx.x % NTD, tf = blockIdx.x / NTD;
  const int d0 = td * 64 * G1_NA, f0 = tf * 64 * G1_NB;
  const u16* h = (const u16*)(ws + WS_H);
  u16* u = (u16*)(ws + WS_U);
  f32x16 acc[G1_NA][G1_NB] = {};
  gemm_loop<G1_NA, G1_NB, false>(acc, h + d0, DM, w1 + f0, FF, SEQ / 64, lds, wave, lane);
#pragma unroll
  for (int a = 0; a < G1_NA; ++a)
#pragma unroll
    for (int b = 0; b < G1_NB; ++b) {
      const int f = f0 + b * 64 + 32 * wn + (lane & 31);
      const int db = d0 + a * 64 + 32 * wm + 4 * (lane >> 5);
      const float bias = bf2f(b1[f]);
      float r[16];
#pragma unroll
      for (int i = 0; i < 16; ++i) {
        const float t = acc[a][b][i] + bias;
        const float z = 0.7978845608028654f * (t + 0.044715f * t * t * t);
        r[i] = t / (1.f + __expf(-2.f * z));
      }
#pragma unroll
      for (int g = 0; g < 4; ++g) {
        u32x2 v = {pk2(r[4 * g], r[4 * g + 1]), pk2(r[4 * g + 2], r[4 * g + 3])};
        *(u32x2*)(u + (size_t)f * DM + db + 8 * g) = v;
      }
    }
}

constexpr int S3_NA = 1, S3_NB = 1;
constexpr int S3_LDS = 3 * (S3_NA + S3_NB) * 8192;
__global__ __launch_bounds__(256) void stage3_kernel(const u16* __restrict__ wo, const u16* __restrict__ w2,
                                                     const u16* __restrict__ b2, const char* __restrict__ ws,
                                                     u16* __restrict__ out) {
  extern __shared__ __attribute__((aligned(16))) char lds[];
  const int tid = threadIdx.x, wave = tid >> 6, lane = tid & 63;
  const int wm = wave >> 1, wn = wave & 1;
  constexpr int NTD = DM / (64 * S3_NA);
  const int td = blockIdx.x % NTD, ts = blockIdx.x / NTD;
  const int d0 = td * 64 * S3_NA, s0 = ts * 64 * S3_NB;
  const u16* o = (const u16*)(ws + WS_O);
  const u16* u = (const u16*)(ws + WS_U);
  const float* gate = (const float*)(ws + WS_G);
  f32x16 acca[S3_NA][S3_NB] = {};
  f32x16 accb[S3_NA][S3_NB] = {};
  gemm_loop<S3_NA, S3_NB, true>(acca, wo + d0, DM, o + (size_t)s0 * DM, DM, DM / 64, lds, wave, lane);
  gemm_loop<S3_NA, S3_NB, false>(accb, u + d0, DM, w2 + s0, SEQ, FF / 64, lds, wave, lane);
#pragma unroll
  for (int a = 0; a < S3_NA; ++a)
#pragma unroll
    for (int b = 0; b < S3_NB; ++b) {
      const int s = s0 + b * 64 + 32 * wn + (lane & 31);
      const int db = d0 + a * 64 + 32 * wm + 4 * (lane >> 5);
      const float g = gate[s], bias = bf2f(b2[s]);
      float r[16];
#pragma unroll
      for (int i = 0; i < 16; ++i) r[i] = g * acca[a][b][i] + (1.f - g) * (accb[a][b][i] + bias);
#pragma unroll
      for (int gg = 0; gg < 4; ++gg) {
        u32x2 v = {pk2(r[4 * gg], r[4 * gg + 1]), pk2(r[4 * gg + 2], r[4 * gg + 3])};
        *(u32x2*)(out + (size_t)s * DM + db + 8 * gg) = v;
      }
    }
}

extern "C" void kernel_launch(void* const* d_in, const int* in_sizes, int n_in, void* d_out, int out_size, void* d_ws,
                              size_t ws_size, hipStream_t stream) {
  const u16* x = (const u16*)d_in[0];
  const u16* wq = (const u16*)d_in[1];
  const u16* wk = (const u16*)d_in[2];
  const u16* wv = (const u16*)d_in[3];
  const u16* wo = (const u16*)d_in[4];
  const u16* w1 = (const u16*)d_in[6];
  const u16* b1 = (const u16*)d_in[7];
  const u16* w2 = (const u16*)d_in[8];
  const u16* b2 = (const u16*)d_in[9];
  const u16* gw = (const u16*)d_in[10];
  char* ws = (char*)d_ws;
  hipFuncSetAttribute((const void*)stage1_kernel, hipFuncAttributeMaxDynamicSharedMemorySize, S1_LDS);
  stage1_kernel<<<S1_QT + S1_KVT + S1_LNT, 256, S1_LDS, stream>>>(x, wq, wk, wv, gw, ws);
  attn_kernel<<<SEQ / AT_WAVES, AT_WAVES * 64, AT_LDS, stream>>>(ws);
  gemm1_kernel<<<(DM / (64 * G1_NA)) * (FF / (64 * G1_NB)), 256, G1_LDS, stream>>>(w1, b1, ws);
  stage3_kernel<<<(DM / (64 * S3_NA)) * (SEQ / (64 * S3_NB)), 256, S3_LDS, stream>>>(wo, w2, b2, ws, (u16*)d_out);
}
```

```cpp
#include <hip/hip_runtime.h>
#include <hip/hip_bf16.h>
#include <stdint.h>

typedef short bf16x8 __attribute__((ext_vector_type(8)));
typedef short bf16x4 __attribute__((ext_vector_type(4)));
typedef float f32x16 __attribute__((ext_vector_type(16)));
typedef float f32x4 __attribute__((ext_vector_type(4)));
typedef float f32x2 __attribute__((ext_vector_type(2)));
typedef __bf16 bfx2 __attribute__((ext_vector_type(2)));
typedef unsigned u32x4 __attribute__((ext_vector_type(4)));
typedef unsigned u32x2 __attribute__((ext_vector_type(2)));
typedef unsigned short u16;
typedef __attribute__((address_space(3))) bf16x4* lds_b4p;

#define DI __device__ __forceinline__
#define MFMA32(a, b, c) __builtin_amdgcn_mfma_f32_32x32x16_bf16((a), (b), (c), 0, 0, 0)
#define MFMA16(a, b, c) __builtin_amdgcn_mfma_f32_16x16x32_bf16((a), (b), (c), 0, 0, 0)

constexpr int SEQ = 2048, DM = 640, NH = 32, DH = 20, FF = 1536;
constexpr int KVCH = 128;
constexpr int KV_KBYTES = KVCH * 48;
constexpr int KV_VSTR = 672;
constexpr int KV_CHBYTES = 12288;
constexpr int NCH = SEQ / KVCH;

constexpr size_t WS_Q = 0;
constexpr size_t WS_H = WS_Q + (size_t)SEQ * DM * 2;
constexpr size_t WS_O = WS_H + (size_t)SEQ * DM * 2;
constexpr size_t WS_U = WS_O + (size_t)SEQ * DM * 2;
constexpr size_t WS_KV = WS_U + (size_t)FF * DM * 2;
constexpr size_t WS_G = WS_KV + (size_t)NCH * KV_CHBYTES;

DI unsigned pk2(float lo, float hi) {
  f32x2 v = {lo, hi};
  bfx2 b = __builtin_convertvector(v, bfx2);
  return __builtin_bit_cast(unsigned, b);
}
DI float bf2f(u16 v) { return __uint_as_float(((unsigned)v) << 16); }
DI u16 f2bf(float f) { return (u16)(pk2(f, 0.f) & 0xffffu); }

DI void glds16(const void* g, unsigned ldsaddr) {
  unsigned keep;
  asm volatile("s_mov_b32 %0, m0\n\ts_mov_b32 m0, %2\n\ts_nop 0\n\tglobal_load_lds_dwordx4 %1, off\n\ts_mov_b32 m0, %0"
               : "=&s"(keep)
               : "v"(g), "s"(ldsaddr)
               : "memory");
}
DI unsigned lds_addr(const void* p) { return (unsigned)(size_t)p; }

#define WAITV(n) asm volatile("s_waitcnt vmcnt(" #n ")" ::: "memory")
#define BARRIER() do { asm volatile("s_waitcnt lgkmcnt(0)" ::: "memory"); __builtin_amdgcn_s_barrier(); asm volatile("" ::: "memory"); } while (0)

DI bf16x8 tr_pair(const char* p0, const char* p1) {
  bf16x4 lo = __builtin_amdgcn_ds_read_tr16_b64_v4i16((lds_b4p)(p0));
  bf16x4 hi = __builtin_amdgcn_ds_read_tr16_b64_v4i16((lds_b4p)(p1));
  return __builtin_shufflevector(lo, hi, 0, 1, 2, 3, 4, 5, 6, 7);
}

constexpr int GSTG = 18432;
DI int colswz(int r) { return (((r >> 1) & 1) | (((r >> 3) & 1) << 1)) << 1; }

template <bool BROW>
struct GemmSrc {
  const u16* p[5];
};

template <bool BROW, int NST>
DI void gemm_core(f32x4 (&acc)[5], const u16* __restrict__ A, int lda, const u16* __restrict__ B, int ldb, int nk,
                  char* lds, int wave, int lane) {
  const int tid = wave * 64 + lane;
  const u16* src[5];
  {
#pragma unroll
    for (int i = 0; i < 2; ++i) {
      const int c = tid + 256 * i, r = c >> 3, ch = (c & 7) ^ colswz(r);
      src[i] = A + (size_t)r * lda + ch * 8;
    }
#pragma unroll
    for (int i = 0; i < 2; ++i) {
      const int c = tid + 256 * i, r = c >> 3;
      if (BROW) src[2 + i] = B + (size_t)r * ldb + ((c & 7) ^ ((r >> 1) & 7)) * 8;
      else src[2 + i] = B + (size_t)r * ldb + ((c & 7) ^ colswz(r)) * 8;
    }
    const int pos = (tid & 127) >> 1, r = pos ^ (((pos >> 3) & 1) << 2);
    src[4] = A + (size_t)r * lda + 64 + (tid & 1) * 8;
  }
  const size_t astep = (size_t)64 * lda, bstep = BROW ? (size_t)64 : (size_t)64 * ldb;
  const unsigned lbase = lds_addr(lds) + wave * 1024;
  auto stage = [&](int buf) {
    const unsigned b = lbase + buf * GSTG;
    glds16(src[0], b);
    glds16(src[1], b + 4096);
    glds16(src[2], b + 10240);
    glds16(src[3], b + 10240 + 4096);
    if (wave < 2) glds16(src[4], b + 8192);
    src[0] += astep; src[1] += astep; src[4] += astep;
    src[2] += bstep; src[3] += bstep;
  };
  const int g = lane >> 4, q = (lane & 15) >> 2, p = lane & 3;
  const int kr = 8 * g + q;
  const int aoff = kr * 128 + 8 * p;
  const int asw = colswz(kr);
  const int a2off = 8192 + (kr ^ (((kr >> 3) & 1) << 2)) * 32 + 8 * p;
  int boff;
  if (BROW) {
    const int row = 16 * wave + (lane & 15);
    boff = 10240 + row * 128;
  } else {
    boff = 10240 + kr * 128 + (((2 * wave) ^ asw) << 4) + 8 * p;
  }
  const int bsw = ((16 * wave + (lane & 15)) >> 1) & 7;

#pragma unroll
  for (int i = 0; i < NST - 1; ++i)
    if (i < nk) stage(i);
  int buf = 0;
  for (int kt = 0; kt < nk; ++kt) {
    const int rem = nk - 1 - kt;
    if (NST == 2) {
      WAITV(0);
    } else if (rem >= NST - 2) {
      if (wave < 2) { if (NST == 3) WAITV(5); else if (NST == 4) WAITV(10); else WAITV(15); }
      else { if (NST == 3) WAITV(4); else if (NST == 4) WAITV(8); else WAITV(12); }
    } else if (NST >= 4 && rem == 1) {
      if (wave < 2) WAITV(5); else WAITV(4);
    } else if (NST >= 5 && rem == 2) {
      if (wave < 2) WAITV(10); else WAITV(8);
    } else {
      WAITV(0);
    }
    BARRIER();
    if (kt + NST - 1 < nk) stage(buf == 0 ? NST - 1 : buf - 1);
    const char* base = lds + buf * GSTG;
#pragma unroll
    for (int ks = 0; ks < 2; ++ks) {
      bf16x8 fa[5], fb;
#pragma unroll
      for (int ct = 0; ct < 4; ++ct) {
        const char* a = base + ks * 4096 + aoff + (((2 * ct) ^ asw) << 4);
        fa[ct] = tr_pair(a, a + 512);
      }
      {
        const char* a = base + ks * 1024 + a2off;
        fa[4] = tr_pair(a, base + ks * 1024 + (a2off ^ 128));
      }
      if (BROW) fb = *(const bf16x8*)(base + boff + (((4 * ks + g) ^ bsw) << 4));
      else fb = tr_pair(base + ks * 4096 + boff, base + ks * 4096 + boff + 512);
#pragma unroll
      for (int ct = 0; ct < 5; ++ct) acc[ct] = MFMA16(fa[ct], fb, acc[ct]);
    }
    buf = buf == NST - 1 ? 0 : buf + 1;
  }
  BARRIER();
}

constexpr int S1_NST = 4;
constexpr int S1_QT = 256;
constexpr int S1_KVT = SEQ / 64;
constexpr int S1_LNT = SEQ / 16;
constexpr int S1_LDS = 640 * 80 + 1024 + 3 * 8192;

DI void stage_row32(const u16* src, int ld, unsigned img, int wave, int lane) {
#pragma unroll
  for (int i = 0; i < 2; ++i) {
    const int row = (wave * 2 + i) * 8 + (lane >> 3);
    const int c = (lane & 7) ^ ((row >> 1) & 7);
    glds16(src + (size_t)row * ld + c * 8, img + (wave * 2 + i) * 1024);
  }
}

__global__ __launch_bounds__(256) void stage1_kernel(const u16* __restrict__ x, const u16* __restrict__ wq,
                                                     const u16* __restrict__ wk, const u16* __restrict__ wv,
                                                     const u16* __restrict__ gw, char* __restrict__ ws) {
  extern __shared__ __attribute__((aligned(16))) char lds[];
  const int tid = threadIdx.x, lane = tid & 63;
  const int wave = __builtin_amdgcn_readfirstlane(tid >> 6);
  int bid = blockIdx.x;
  if (bid < S1_QT) {
    const int xcd = bid & 7, j = bid >> 3;
    const int ts = xcd * 4 + (j >> 3), tn = j & 7;
    const int n0 = tn * 80, s0 = ts * 64;
    f32x4 acc[5] = {};
    gemm_core<true, S1_NST>(acc, wq + n0, DM, x + (size_t)s0 * DM, DM, DM / 64, lds, wave, lane);
    const float qs = 1.4426950408889634f * 0.22360679774997896f;
    u16* q = (u16*)(ws + WS_Q);
    const int s = s0 + 16 * wave + (lane & 15);
#pragma unroll
    for (int ct = 0; ct < 5; ++ct) {
      u32x2 v = {pk2(acc[ct][0] * qs, acc[ct][1] * qs), pk2(acc[ct][2] * qs, acc[ct][3] * qs)};
      *(u32x2*)(q + (size_t)s * DM + n0 + 16 * ct + 4 * (lane >> 4)) = v;
    }
    return;
  }
  bid -= S1_QT;
  if (bid < S1_KVT) {
    const int wm = wave >> 1, wn = wave & 1;
    const int s0 = bid * 64;
    char* wimg = lds;
    char* aimg = lds + 640 * 80 + 1024;
    stage_row32(x + (size_t)s0 * DM, DM, lds_addr(aimg), wave, lane);
    stage_row32(x + (size_t)s0 * DM + 64, DM, lds_addr(aimg) + 8192, wave, lane);
    for (int i = tid; i < 640 * 10; i += 256) {
      const int k = i / 10, pc = i % 10;
      const u16* src = pc < 5 ? (wk + k * DH + pc * 4) : (wv + k * DH + (pc - 5) * 4);
      *(u32x2*)(wimg + k * 80 + pc * 8) = *(const u32x2*)src;
    }
    *(unsigned*)(wimg + 640 * 80 + tid * 4) = 0u;
    f32x16 acc = {};
    int buf = 0;
    const int i16 = lane & 15, q4 = i16 >> 2, p4 = i16 & 3, hh = lane >> 5, g1 = (lane >> 4) & 1;
    for (int kt = 0; kt < 10; ++kt) {
      if (kt + 1 < 10) WAITV(2); else WAITV(0);
      BARRIER();
      if (kt + 2 < 10)
        stage_row32(x + (size_t)s0 * DM + (kt + 2) * 64, DM, lds_addr(aimg) + (buf == 0 ? 2 : buf - 1) * 8192, wave, lane);
#pragma unroll
      for (int kk = 0; kk < 4; ++kk) {
        const int krow = kt * 64 + 16 * kk + 8 * hh + q4;
        const char* ap = wimg + krow * 80 + (32 * wm + 16 * g1 + 4 * p4) * 2;
        bf16x8 fa = tr_pair(ap, ap + 4 * 80);
        const int row = 32 * wn + (lane & 31);
        const int c = 2 * kk + hh;
        bf16x8 fb = *(const bf16x8*)(aimg + buf * 8192 + row * 128 + ((c ^ ((row >> 1) & 7)) << 4));
        acc = MFMA32(fa, fb, acc);
      }
      buf = buf == 2 ? 0 : buf + 1;
    }
    const int s = s0 + 32 * wn + (lane & 31);
    char* kvp = ws + WS_KV + (size_t)(s / KVCH) * KV_CHBYTES;
    const int key = s % KVCH;
    const int kb = key >> 4, r = key & 15, vh = (r >> 2) & 1, vj = (r & 3) + 4 * (r >> 3);
    char* vbase = kvp + KV_KBYTES + kb * KV_VSTR + vh * 336 + vj * 2;
#pragma unroll
    for (int g = 0; g < 4; ++g) {
      const int c0 = 32 * wm + 8 * g + 4 * hh;
      if (c0 < 20) {
        u32x2 v = {pk2(acc[4 * g], acc[4 * g + 1]), pk2(acc[4 * g + 2], acc[4 * g + 3])};
        *(u32x2*)(kvp + key * 48 + c0 * 2) = v;
      } else if (c0 < 40) {
#pragma unroll
        for (int j = 0; j < 4; ++j) *(u16*)(vbase + (c0 - 20 + j) * 16) = f2bf(acc[4 * g + j]);
      }
    }
    if (wm == 0 && hh == 0) {
      u32x2 v = {0x00003f80u, 0u};
      *(u32x2*)(kvp + key * 48 + 40) = v;
      *(u16*)(vbase + 20 * 16) = 0x3f80;
    }
    return;
  }
  bid -= S1_KVT;
  {
    u16* h = (u16*)(ws + WS_H);
    float* gate = (float*)(ws + WS_G);
    float gwv[10];
#pragma unroll
    for (int i = 0; i < 5; ++i) {
      const unsigned u = *(const unsigned*)(gw + (lane + 64 * i) * 2);
      gwv[2 * i] = __uint_as_float(u << 16);
      gwv[2 * i + 1] = __uint_as_float(u & 0xffff0000u);
    }
#pragma unroll
    for (int rr = 0; rr < 4; ++rr) {
      const int s = bid * 16 + wave * 4 + rr;
      float v[10];
      float sum = 0.f, dot = 0.f;
#pragma unroll
      for (int i = 0; i < 5; ++i) {
        const unsigned u = *(const unsigned*)(x + (size_t)s * DM + (lane + 64 * i) * 2);
        v[2 * i] = __uint_as_float(u << 16);
        v[2 * i + 1] = __uint_as_float(u & 0xffff0000u);
        sum += v[2 * i] + v[2 * i + 1];
        dot += v[2 * i] * gwv[2 * i] + v[2 * i + 1] * gwv[2 * i + 1];
      }
#pragma unroll
      for (int o = 32; o >= 1; o >>= 1) { sum += __shfl_xor(sum, o); dot += __shfl_xor(dot, o); }
      const float mean = sum * (1.f / DM);
      float var = 0.f;
#pragma unroll
      for (int i = 0; i < 10; ++i) { const float d = v[i] - mean; var += d * d; }
#pragma unroll
      for (int o = 32; o >= 1; o >>= 1) var += __shfl_xor(var, o);
      const float rstd = 1.f / sqrtf(var * (1.f / DM) + 1e-5f);
#pragma unroll
      for (int i = 0; i < 5; ++i)
        *(unsigned*)(h + (size_t)s * DM + (lane + 64 * i) * 2) = pk2((v[2 * i] - mean) * rstd, (v[2 * i + 1] - mean) * rstd);
      if (lane == 0) gate[s] = 1.f / (1.f + __expf(-dot));
    }
  }
}

constexpr int G1_WGS = 192;
constexpr int AT_WGS = SEQ / 4;
constexpr int G1_NST = 2;
constexpr int S2_FLAG = 4 * KV_CHBYTES;
constexpr int S2_LDS = S2_FLAG + 64;

template <bool SAFE>
DI void attn_pass(const char* __restrict__ ws, char* lds, int srow, f32x16& o, float& shift, int wave, int lane) {
  const int hd = lane & 31, hh = lane >> 5;
  const int half = wave >> 2;
  const char* kvg = ws + WS_KV;
  bf16x8 qf0, qf1;
  {
    const u16* q = (const u16*)(ws + WS_Q) + (size_t)srow * DM + hd * DH;
    u32x2 a = *(const u32x2*)(q + 8 * hh), b = *(const u32x2*)(q + 8 * hh + 4), c = *(const u32x2*)(q + 16);
    u32x4 t0 = {a[0], a[1], b[0], b[1]};
    u32x4 t1 = {hh == 0 ? c[0] : 0u, hh == 0 ? c[1] : 0u, 0u, 0u};
    qf0 = __builtin_bit_cast(bf16x8, t0);
    qf1 = __builtin_bit_cast(bf16x8, t1);
  }
  const int koff0 = half * KV_CHBYTES + hd * 48 + 16 * hh, koff1 = half * KV_CHBYTES + hd * 48 + 32;
  const int voff = half * KV_CHBYTES + KV_KBYTES + hh * 336 + (hd < 20 ? hd : 20) * 16;
  const char* gsrc = kvg + (size_t)(half * (NCH / 2)) * KV_CHBYTES + (wave & 3) * 3072 + lane * 16;
  const unsigned ldst = lds_addr(lds) + wave * 3072;
  auto stage = [&](int buf) {
#pragma unroll
    for (int i = 0; i < 3; ++i) glds16(gsrc + i * 1024, ldst + buf * (2 * KV_CHBYTES) + i * 1024);
    gsrc += KV_CHBYTES;
  };
  float mrun = -3.0e38f;
  stage(0);
  for (int ch = 0; ch < NCH / 2; ++ch) {
    WAITV(0);
    BARRIER();
    if (ch + 1 < NCH / 2) stage((ch + 1) & 1);
    const char* base = lds + (ch & 1) * (2 * KV_CHBYTES);
    bf16x8 k0 = *(const bf16x8*)(base + koff0);
    bf16x8 k1 = *(const bf16x8*)(base + koff1);
    bf16x8 v0 = *(const bf16x8*)(base + voff);
    bf16x8 v1 = *(const bf16x8*)(base + voff + KV_VSTR);
    if (!SAFE && ch == 0) {
      f32x16 z = {};
      f32x16 s = MFMA32(k0, qf0, z);
      s = MFMA32(k1, qf1, s);
      float m = s[0];
#pragma unroll
      for (int i = 1; i < 16; ++i) m = fmaxf(m, s[i]);
      m = fmaxf(m, __shfl_xor(m, 32));
      const u16 mb = f2bf(m);
      shift = bf2f(mb);
      if (hh == 0) qf1[4] = (short)(mb ^ 0x8000u);
    }
#pragma unroll SAFE ? 1 : 4
    for (int t = 0; t < KVCH / 32; ++t) {
      f32x16 z = {};
      f32x16 s = MFMA32(k0, qf0, z);
      s = MFMA32(k1, qf1, s);
      bf16x8 cv0 = v0, cv1 = v1;
      if (t + 1 < KVCH / 32) {
        k0 = *(const bf16x8*)(base + (t + 1) * 32 * 48 + koff0);
        k1 = *(const bf16x8*)(base + (t + 1) * 32 * 48 + koff1);
        v0 = *(const bf16x8*)(base + voff + (2 * t + 2) * KV_VSTR);
        v1 = *(const bf16x8*)(base + voff + (2 * t + 3) * KV_VSTR);
      }
      if (SAFE) {
        float m = s[0];
#pragma unroll
        for (int i = 1; i < 16; ++i) m = fmaxf(m, s[i]);
        m = fmaxf(m, __shfl_xor(m, 32));
        const float mn = fmaxf(mrun, m);
        const float alpha = __builtin_amdgcn_exp2f(mrun - mn);
        mrun = mn;
#pragma unroll
        for (int i = 0; i < 16; ++i) { o[i] *= alpha; s[i] -= mn; }
      }
      u32x4 p0, p1;
#pragma unroll
      for (int i = 0; i < 4; ++i) {
        p0[i] = pk2(__builtin_amdgcn_exp2f(s[2 * i]), __builtin_amdgcn_exp2f(s[2 * i + 1]));
        p1[i] = pk2(__builtin_amdgcn_exp2f(s[8 + 2 * i]), __builtin_amdgcn_exp2f(s[8 + 2 * i + 1]));
      }
      o = MFMA32(cv0, __builtin_bit_cast(bf16x8, p0), o);
      o = MFMA32(cv1, __builtin_bit_cast(bf16x8, p1), o);
    }
  }
  if (SAFE) shift = mrun;
}

__global__ __launch_bounds__(512, 5) void stage2_kernel(const u16* __restrict__ w1, const u16* __restrict__ b1,
                                                        char* __restrict__ ws) {
  extern __shared__ __attribute__((aligned(16))) char lds[];
  const int tid = threadIdx.x, lane = tid & 63;
  const int wave = __builtin_amdgcn_readfirstlane(tid >> 6);
  if (blockIdx.x < G1_WGS) {
    if (wave >= 4) return;
    const int bid = blockIdx.x;
    const int xcd = bid & 7, j = bid >> 3;
    const int tf = xcd * 3 + (j >> 3), td = j & 7;
    const int d0 = td * 80, f0 = tf * 64;
    const u16* h = (const u16*)(ws + WS_H);
    u16* u = (u16*)(ws + WS_U);
    f32x4 acc[5] = {};
    gemm_core<false, G1_NST>(acc, h + d0, DM, w1 + f0, FF, SEQ / 64, lds, wave, lane);
    const int f = f0 + 16 * wave + (lane & 15);
    const float bias = bf2f(b1[f]);
#pragma unroll
    for (int ct = 0; ct < 5; ++ct) {
      float r[4];
#pragma unroll
      for (int i = 0; i < 4; ++i) {
        const float t = acc[ct][i] + bias;
        const float z = 0.7978845608028654f * (t + 0.044715f * t * t * t);
        r[i] = t / (1.f + __expf(-2.f * z));
      }
      u32x2 v = {pk2(r[0], r[1]), pk2(r[2], r[3])};
      *(u32x2*)(u + (size_t)f * DM + d0 + 16 * ct + 4 * (lane >> 4)) = v;
    }
    return;
  }
  const int half = wave >> 2;
  const int s = (blockIdx.x - G1_WGS) * 4 + (wave & 3);
  f32x16 o = {};
  float shift = 0.f;
  volatile unsigned* flag = (volatile unsigned*)(lds + S2_FLAG);
  if (tid == 0) *flag = 0u;
  attn_pass<false>(ws, lds, s, o, shift, wave, lane);
  int lane2 = lane;
  asm volatile("" : "+v"(lane2));
  const int hd = lane2 & 31, hh = lane2 >> 5;
  {
    const float l = __shfl(o[8], hd + 32);
    const bool bad = !(l > 8.67e-19f && l < 1.15e18f);
    if (bad) *flag = 1u;
    __syncthreads();
    if (__builtin_amdgcn_readfirstlane(*flag) != 0u) {
      o = f32x16{};
      attn_pass<true>(ws, lds, s, o, shift, wave, lane2);
    }
  }
  __syncthreads();
  float* mg = (float*)lds + (wave & 3) * 13 * 64;
  if (half == 1) {
#pragma unroll
    for (int i = 0; i < 12; ++i) mg[i * 64 + lane2] = o[i];
    mg[12 * 64 + lane2] = shift;
  }
  __syncthreads();
  if (half == 0) {
    const float sh1 = mg[12 * 64 + lane2];
    const float mm = fmaxf(shift, sh1);
    const float f0 = __builtin_amdgcn_exp2f(shift - mm), f1 = __builtin_amdgcn_exp2f(sh1 - mm);
#pragma unroll
    for (int i = 0; i < 12; ++i) o[i] = o[i] * f0 + mg[i * 64 + lane2] * f1;
    const float l = __shfl(o[8], hd + 32);
    const float inv = 1.f / l;
    u16* op = (u16*)(ws + WS_O) + (size_t)s * DM + hd * DH + 4 * hh;
#pragma unroll
    for (int g = 0; g < 3; ++g) {
      if (g < 2 || hh == 0) {
        u32x2 v = {pk2(o[4 * g] * inv, o[4 * g + 1] * inv), pk2(o[4 * g + 2] * inv, o[4 * g + 3] * inv)};
        *(u32x2*)(op + 8 * g) = v;
      }
    }
  }
}

constexpr int S3_NST = 5;
constexpr int S3_LDS = S3_NST * GSTG;
__global__ __launch_bounds__(256) void stage3_kernel(const u16* __restrict__ wo, const u16* __restrict__ w2,
                                                     const u16* __restrict__ b2, const char* __restrict__ ws,
                                                     u16* __restrict__ out) {
  extern __shared__ __attribute__((aligned(16))) char lds[];
  const int tid = threadIdx.x, lane = tid & 63;
  const int wave = __builtin_amdgcn_readfirstlane(tid >> 6);
  const int bid = blockIdx.x;
  const int xcd = bid & 7, j = bid >> 3;
  const int ts = xcd * 4 + (j >> 3), td = j & 7;
  const int d0 = td * 80, s0 = ts * 64;
  const u16* o = (const u16*)(ws + WS_O);
  const u16* u = (const u16*)(ws + WS_U);
  const float* gate = (const float*)(ws + WS_G);
  f32x4 acca[5] = {};
  f32x4 accb[5] = {};
  gemm_core<false, S3_NST>(accb, u + d0, DM, w2 + s0, SEQ, FF / 64, lds, wave, lane);
  gemm_core<true, S3_NST>(acca, wo + d0, DM, o + (size_t)s0 * DM, DM, DM / 64, lds, wave, lane);
  const int s = s0 + 16 * wave + (lane & 15);
  const float g = gate[s], bias = bf2f(b2[s]);
#pragma unroll
  for (int ct = 0; ct < 5; ++ct) {
    float r[4];
#pragma unroll
    for (int i = 0; i < 4; ++i) r[i] = g * acca[ct][i] + (1.f - g) * (accb[ct][i] + bias);
    u32x2 v = {pk2(r[0], r[1]), pk2(r[2], r[3])};
    *(u32x2*)(out + (size_t)s * DM + d0 + 16 * ct + 4 * (lane >> 4)) = v;
  }
}

extern "C" void kernel_launch(void* const* d_in, const int* in_sizes, int n_in, void* d_out, int out_size, void* d_ws,
                              size_t ws_size, hipStream_t stream) {
  const u16* x = (const u16*)d_in[0];
  const u16* wq = (const u16*)d_in[1];
  const u16* wk = (const u16*)d_in[2];
  const u16* wv = (const u16*)d_in[3];
  const u16* wo = (const u16*)d_in[4];
  const u16* w1 = (const u16*)d_in[6];
  const u16* b1 = (const u16*)d_in[7];
  const u16* w2 = (const u16*)d_in[8];
  const u16* b2 = (const u16*)d_in[9];
  const u16* gw = (const u16*)d_in[10];
  char* ws = (char*)d_ws;
  (void)hipFuncSetAttribute((const void*)stage1_kernel, hipFuncAttributeMaxDynamicSharedMemorySize, S1_LDS);
  (void)hipFuncSetAttribute((const void*)stage3_kernel, hipFuncAttributeMaxDynamicSharedMemorySize, S3_LDS);
  stage1_kernel<<<S1_QT + S1_KVT + S1_LNT, 256, S1_LDS, stream>>>(x, wq, wk, wv, gw, ws);
  stage2_kernel<<<G1_WGS + AT_WGS, 512, S2_LDS, stream>>>(w1, b1, ws);
  stage3_kernel<<<256, 256, S3_LDS, stream>>>(wo, w2, b2, ws, (u16*)d_out);
}
```

```cpp
#include <hip/hip_runtime.h>
#include <hip/hip_bf16.h>
#include <stdint.h>

typedef short bf16x8 __attribute__((ext_vector_type(8)));
typedef short bf16x4 __attribute__((ext_vector_type(4)));
typedef float f32x16 __attribute__((ext_vector_type(16)));
typedef float f32x4 __attribute__((ext_vector_type(4)));
typedef float f32x2 __attribute__((ext_vector_type(2)));
typedef __bf16 bfx2 __attribute__((ext_vector_type(2)));
typedef unsigned u32x4 __attribute__((ext_vector_type(4)));
typedef unsigned u32x2 __attribute__((ext_vector_type(2)));
typedef unsigned short u16;
typedef __attribute__((address_space(3))) bf16x4* lds_b4p;

#define DI __device__ __forceinline__
#define MFMA32(a, b, c) __builtin_amdgcn_mfma_f32_32x32x16_bf16((a), (b), (c), 0, 0, 0)
#define MFMA16(a, b, c) __builtin_amdgcn_mfma_f32_16x16x32_bf16((a), (b), (c), 0, 0, 0)

constexpr int SEQ = 2048, DM = 640, NH = 32, DH = 20, FF = 1536;
constexpr int KVCH = 128;
constexpr int KV_KBYTES = KVCH * 48;
constexpr int KV_VSTR = 672;
constexpr int KV_CHBYTES = 12288;
constexpr int NCH = SEQ / KVCH;

constexpr size_t WS_Q = 0;
constexpr size_t WS_H = WS_Q + (size_t)SEQ * DM * 2;
constexpr size_t WS_O = WS_H + (size_t)SEQ * DM * 2;
constexpr size_t WS_U = WS_O + (size_t)SEQ * DM * 2;
constexpr size_t WS_KV = WS_U + (size_t)FF * DM * 2;
constexpr size_t WS_G = WS_KV + (size_t)NCH * KV_CHBYTES;

DI unsigned pk2(float lo, float hi) {
  f32x2 v = {lo, hi};
  bfx2 b = __builtin_convertvector(v, bfx2);
  return __builtin_bit_cast(unsigned, b);
}
DI float bf2f(u16 v) { return __uint_as_float(((unsigned)v) << 16); }
DI u16 f2bf(float f) { return (u16)(pk2(f, 0.f) & 0xffffu); }

DI void glds16(const void* g, unsigned ldsaddr) {
  unsigned keep;
  asm volatile("s_mov_b32 %0, m0\n\ts_mov_b32 m0, %2\n\ts_nop 0\n\tglobal_load_lds_dwordx4 %1, off\n\ts_mov_b32 m0, %0"
               : "=&s"(keep)
               : "v"(g), "s"(ldsaddr)
               : "memory");
}
DI unsigned lds_addr(const void* p) { return (unsigned)(size_t)p; }

#define WAITV(n) asm volatile("s_waitcnt vmcnt(" #n ")" ::: "memory")
#define BARRIER() do { asm volatile("s_waitcnt lgkmcnt(0)" ::: "memory"); __builtin_amdgcn_s_barrier(); asm volatile("" ::: "memory"); } while (0)

DI bf16x8 tr_pair(const char* p0, const char* p1) {
  bf16x4 lo = __builtin_amdgcn_ds_read_tr16_b64_v4i16((lds_b4p)(p0));
  bf16x4 hi = __builtin_amdgcn_ds_read_tr16_b64_v4i16((lds_b4p)(p1));
  return __builtin_shufflevector(lo, hi, 0, 1, 2, 3, 4, 5, 6, 7);
}

constexpr int GSTG = 18432;
template <int NCT> constexpr int gboff() { return NCT == 5 ? 10240 : 8192; }
template <int NCT> constexpr int gstg() { return gboff<NCT>() + 8192; }
DI int colswz(int r) { return (((r >> 1) & 1) | (((r >> 3) & 1) << 1)) << 1; }

struct GemmSeg {
  const u16* src[5];
  size_t astep, bstep;
  int nk, kcur;
};
template <bool BROW>
DI void seg_init(GemmSeg& sg, const u16* A, int lda, const u16* B, int ldb, int nk, int k0, int tid) {
  sg.astep = (size_t)64 * lda;
  sg.bstep = BROW ? (size_t)64 : (size_t)64 * ldb;
  sg.nk = nk;
  sg.kcur = k0;
  A += (size_t)k0 * sg.astep;
  B += (size_t)k0 * sg.bstep;
#pragma unroll
  for (int i = 0; i < 2; ++i) {
    const int c = tid + 256 * i, r = c >> 3, ch = (c & 7) ^ colswz(r);
    sg.src[i] = A + (size_t)r * lda + ch * 8;
    if (BROW) sg.src[2 + i] = B + (size_t)r * ldb + ((c & 7) ^ ((r >> 1) & 7)) * 8;
    else sg.src[2 + i] = B + (size_t)r * ldb + ch * 8;
  }
  const int pos = (tid & 127) >> 1, r = pos ^ (((pos >> 3) & 1) << 2);
  sg.src[4] = A + (size_t)r * lda + 64 + (tid & 1) * 8;
}
template <int NCT>
DI void seg_stage(GemmSeg& sg, unsigned b  , int wave) {
  glds16(sg.src[0], b);
  glds16(sg.src[1], b + 4096);
  glds16(sg.src[2], b + gboff<NCT>());
  glds16(sg.src[3], b + gboff<NCT>() + 4096);
  if (NCT == 5 && wave < 2) glds16(sg.src[4], b + 8192);
  sg.kcur += 1;
  if (sg.kcur == sg.nk) {
    sg.kcur = 0;
    const size_t ab = sg.astep * (sg.nk - 1), bb = sg.bstep * (sg.nk - 1);
    sg.src[0] -= ab; sg.src[1] -= ab; sg.src[4] -= ab;
    sg.src[2] -= bb; sg.src[3] -= bb;
  } else {
    sg.src[0] += sg.astep; sg.src[1] += sg.astep; sg.src[4] += sg.astep;
    sg.src[2] += sg.bstep; sg.src[3] += sg.bstep;
  }
}
struct FragAddr { int aoff, asw, a2off, boffc, boffr, bsw, g; };
DI FragAddr frag_addr(int wave, int lane) {
  FragAddr f;
  const int g = lane >> 4, q = (lane & 15) >> 2, p = lane & 3;
  const int kr = 8 * g + q;
  f.g = g;
  f.aoff = kr * 128 + 8 * p;
  f.asw = colswz(kr);
  f.a2off = 8192 + (kr ^ (((kr >> 3) & 1) << 2)) * 32 + 8 * p;
  const int row = 16 * wave + (lane & 15);
  f.boffr = row * 128;
  f.boffc = kr * 128 + (((2 * wave) ^ f.asw) << 4) + 8 * p;
  f.bsw = (row >> 1) & 7;
  return f;
}
template <bool BROW, int NCT>
DI void gemm_step(f32x4 (&acc)[5], const char* base, const FragAddr& f) {
#pragma unroll
  for (int ks = 0; ks < 2; ++ks) {
    bf16x8 fa[5], fb;
#pragma unroll
    for (int ct = 0; ct < 4; ++ct) {
      const char* a = base + ks * 4096 + f.aoff + (((2 * ct) ^ f.asw) << 4);
      fa[ct] = tr_pair(a, a + 512);
    }
    if (NCT == 5) fa[4] = tr_pair(base + ks * 1024 + f.a2off, base + ks * 1024 + (f.a2off ^ 128));
    const char* bb = base + gboff<NCT>();
    if (BROW) fb = *(const bf16x8*)(bb + f.boffr + (((4 * ks + f.g) ^ f.bsw) << 4));
    else fb = tr_pair(bb + ks * 4096 + f.boffc, bb + ks * 4096 + f.boffc + 512);
#pragma unroll
    for (int ct = 0; ct < NCT; ++ct) acc[ct] = MFMA16(fa[ct], fb, acc[ct]);
  }
}
template <int NCT>
DI void wait_stages(int n, int wave) {
  if (NCT == 5 && wave < 2) {
    if (n >= 4) WAITV(20); else if (n == 3) WAITV(15); else if (n == 2) WAITV(10); else if (n == 1) WAITV(5); else WAITV(0);
  } else {
    if (n >= 4) WAITV(16); else if (n == 3) WAITV(12); else if (n == 2) WAITV(8); else if (n == 1) WAITV(4); else WAITV(0);
  }
}
template <bool BROW, int NCT, int NST>
DI void gemm_core(f32x4 (&acc)[5], const u16* __restrict__ A, int lda, const u16* __restrict__ B, int ldb, int nk,
                  int k0, char* lds, int wave, int lane) {
  GemmSeg sg;
  seg_init<BROW>(sg, A, lda, B, ldb, nk, k0, wave * 64 + lane);
  const FragAddr f = frag_addr(wave, lane);
  const unsigned lbase = lds_addr(lds) + wave * 1024;
#pragma unroll
  for (int i = 0; i < NST - 1; ++i) seg_stage<NCT>(sg, lbase + i * gstg<NCT>(), wave);
  int buf = 0;
  for (int kt = 0; kt < nk; ++kt) {
    const int rem = nk - 1 - kt;
    wait_stages<NCT>(rem < NST - 2 ? rem : NST - 2, wave);
    BARRIER();
    if (kt + NST - 1 < nk) seg_stage<NCT>(sg, lbase + (buf == 0 ? NST - 1 : buf - 1) * gstg<NCT>(), wave);
    gemm_step<BROW, NCT>(acc, lds + buf * gstg<NCT>(), f);
    buf = buf == NST - 1 ? 0 : buf + 1;
  }
}

constexpr int S1_NST = 4;
constexpr int S1_QT = 256;
constexpr int S1_KVT = SEQ / 64;
constexpr int S1_LNT = SEQ / 16;
constexpr int S1_LDS = 640 * 80 + 1024 + 3 * 8192;

DI void stage_row32(const u16* src, int ld, unsigned img, int wave, int lane) {
#pragma unroll
  for (int i = 0; i < 2; ++i) {
    const int row = (wave * 2 + i) * 8 + (lane >> 3);
    const int c = (lane & 7) ^ ((row >> 1) & 7);
    glds16(src + (size_t)row * ld + c * 8, img + (wave * 2 + i) * 1024);
  }
}

__global__ __launch_bounds__(256) void stage1_kernel(const u16* __restrict__ x, const u16* __restrict__ wq,
                                                     const u16* __restrict__ wk, const u16* __restrict__ wv,
                                                     const u16* __restrict__ gw, char* __restrict__ ws) {
  extern __shared__ __attribute__((aligned(16))) char lds[];
  const int tid = threadIdx.x, lane = tid & 63;
  const int wave = __builtin_amdgcn_readfirstlane(tid >> 6);
  int bid = blockIdx.x;
  if (bid < S1_QT) {
    const int xcd = bid & 7, j = bid >> 3;
    const int ts = xcd * 4 + (j >> 3), tn = j & 7;
    const int n0 = tn * 80, s0 = ts * 64;
    f32x4 acc[5] = {};
    gemm_core<true, 5, S1_NST>(acc, wq + n0, DM, x + (size_t)s0 * DM, DM, DM / 64, (tn + 3 * (j >> 3)) % 10, lds, wave, lane);
    const float qs = 1.4426950408889634f * 0.22360679774997896f;
    u16* q = (u16*)(ws + WS_Q);
    const int s = s0 + 16 * wave + (lane & 15);
#pragma unroll
    for (int ct = 0; ct < 5; ++ct) {
      u32x2 v = {pk2(acc[ct][0] * qs, acc[ct][1] * qs), pk2(acc[ct][2] * qs, acc[ct][3] * qs)};
      *(u32x2*)(q + (size_t)s * DM + n0 + 16 * ct + 4 * (lane >> 4)) = v;
    }
    return;
  }
  bid -= S1_QT;
  if (bid < S1_KVT) {
    const int wm = wave >> 1, wn = wave & 1;
    const int s0 = bid * 64;
    char* wimg = lds;
    char* aimg = lds + 640 * 80 + 1024;
    for (int i = wave; i < 50; i += 4) {
      const char* src = i < 25 ? ((const char*)wk + i * 1024) : ((const char*)wv + (i - 25) * 1024);
      glds16(src + lane * 16, lds_addr(wimg) + i * 1024);
    }
    stage_row32(x + (size_t)s0 * DM, DM, lds_addr(aimg), wave, lane);
    stage_row32(x + (size_t)s0 * DM + 64, DM, lds_addr(aimg) + 8192, wave, lane);
    f32x16 acc = {};
    int buf = 0;
    const int i16 = lane & 15, q4 = i16 >> 2, p4 = i16 & 3, hh = lane >> 5, g1 = (lane >> 4) & 1;
    for (int kt = 0; kt < 10; ++kt) {
      if (kt + 1 < 10) WAITV(2); else WAITV(0);
      BARRIER();
      if (kt + 2 < 10)
        stage_row32(x + (size_t)s0 * DM + (kt + 2) * 64, DM, lds_addr(aimg) + (buf == 0 ? 2 : buf - 1) * 8192, wave, lane);
#pragma unroll
      for (int kk = 0; kk < 4; ++kk) {
        const int krow = kt * 64 + 16 * kk + 8 * hh + q4;
        const char* ap = wimg + wm * 25600 + krow * 40 + (16 * g1 + 4 * p4) * 2;
        bf16x8 fa = tr_pair(ap, ap + 4 * 40);
        const int row = 32 * wn + (lane & 31);
        const int c = 2 * kk + hh;
        bf16x8 fb = *(const bf16x8*)(aimg + buf * 8192 + row * 128 + ((c ^ ((row >> 1) & 7)) << 4));
        acc = MFMA32(fa, fb, acc);
      }
      buf = buf == 2 ? 0 : buf + 1;
    }
    const int s = s0 + 32 * wn + (lane & 31);
    char* kvp = ws + WS_KV + (size_t)(s / KVCH) * KV_CHBYTES;
    const int key = s % KVCH;
    const int kb = key >> 4, r = key & 15, vh = (r >> 2) & 1, vj = (r & 3) + 4 * (r >> 3);
    char* vbase = kvp + KV_KBYTES + kb * KV_VSTR + vh * 336 + vj * 2;
#pragma unroll
    for (int g = 0; g < 4; ++g) {
      const int c0 = 8 * g + 4 * hh;
      if (c0 < 20) {
        if (wm == 0) {
          u32x2 v = {pk2(acc[4 * g], acc[4 * g + 1]), pk2(acc[4 * g + 2], acc[4 * g + 3])};
          *(u32x2*)(kvp + key * 48 + c0 * 2) = v;
        } else {
#pragma unroll
          for (int j = 0; j < 4; ++j) *(u16*)(vbase + (c0 + j) * 16) = f2bf(acc[4 * g + j]);
        }
      }
    }
    if (wm == 0 && hh == 0) {
      u32x2 v = {0x00003f80u, 0u};
      *(u32x2*)(kvp + key * 48 + 40) = v;
      *(u16*)(vbase + 20 * 16) = 0x3f80;
    }
    return;
  }
  bid -= S1_KVT;
  {
    u16* h = (u16*)(ws + WS_H);
    float* gate = (float*)(ws + WS_G);
    float gwv[10];
#pragma unroll
    for (int i = 0; i < 5; ++i) {
      const unsigned u = *(const unsigned*)(gw + (lane + 64 * i) * 2);
      gwv[2 * i] = __uint_as_float(u << 16);
      gwv[2 * i + 1] = __uint_as_float(u & 0xffff0000u);
    }
#pragma unroll
    for (int rr = 0; rr < 4; ++rr) {
      const int s = bid * 16 + wave * 4 + rr;
      float v[10];
      float sum = 0.f, dot = 0.f;
#pragma unroll
      for (int i = 0; i < 5; ++i) {
        const unsigned u = *(const unsigned*)(x + (size_t)s * DM + (lane + 64 * i) * 2);
        v[2 * i] = __uint_as_float(u << 16);
        v[2 * i + 1] = __uint_as_float(u & 0xffff0000u);
        sum += v[2 * i] + v[2 * i + 1];
        dot += v[2 * i] * gwv[2 * i] + v[2 * i + 1] * gwv[2 * i + 1];
      }
#pragma unroll
      for (int o = 32; o >= 1; o >>= 1) { sum += __shfl_xor(sum, o); dot += __shfl_xor(dot, o); }
      const float mean = sum * (1.f / DM);
      float var = 0.f;
#pragma unroll
      for (int i = 0; i < 10; ++i) { const float d = v[i] - mean; var += d * d; }
#pragma unroll
      for (int o = 32; o >= 1; o >>= 1) var += __shfl_xor(var, o);
      const float rstd = 1.f / sqrtf(var * (1.f / DM) + 1e-5f);
#pragma unroll
      for (int i = 0; i < 5; ++i)
        *(unsigned*)(h + (size_t)s * DM + (lane + 64 * i) * 2) = pk2((v[2 * i] - mean) * rstd, (v[2 * i + 1] - mean) * rstd);
      if (lane == 0) gate[s] = 1.f / (1.f + __expf(-dot));
    }
  }
}

constexpr int G1_WGS = 240;
constexpr int AT_WGS = SEQ / 4;
constexpr int G1_NST = 3;
constexpr int S2_FLAG = 4 * KV_CHBYTES;
constexpr int S2_LDS = S2_FLAG + 64;

template <bool SAFE>
DI void attn_pass(const char* __restrict__ ws, char* lds, int srow, f32x16& o, float& shift, int wave, int lane) {
  const int hd = lane & 31, hh = lane >> 5;
  const int half = wave >> 2;
  const char* kvg = ws + WS_KV;
  bf16x8 qf0, qf1;
  {
    const u16* q = (const u16*)(ws + WS_Q) + (size_t)srow * DM + hd * DH;
    u32x2 a = *(const u32x2*)(q + 8 * hh), b = *(const u32x2*)(q + 8 * hh + 4), c = *(const u32x2*)(q + 16);
    u32x4 t0 = {a[0], a[1], b[0], b[1]};
    u32x4 t1 = {hh == 0 ? c[0] : 0u, hh == 0 ? c[1] : 0u, 0u, 0u};
    qf0 = __builtin_bit_cast(bf16x8, t0);
    qf1 = __builtin_bit_cast(bf16x8, t1);
  }
  const int koff0 = half * KV_CHBYTES + hd * 48 + 16 * hh, koff1 = half * KV_CHBYTES + hd * 48 + 32;
  const int voff = half * KV_CHBYTES + KV_KBYTES + hh * 336 + (hd < 20 ? hd : 20) * 16;
  const char* gsrc = kvg + (size_t)(half * (NCH / 2)) * KV_CHBYTES + (wave & 3) * 3072 + lane * 16;
  const unsigned ldst = lds_addr(lds) + wave * 3072;
  auto stage = [&](int buf) {
#pragma unroll
    for (int i = 0; i < 3; ++i) glds16(gsrc + i * 1024, ldst + buf * (2 * KV_CHBYTES) + i * 1024);
    gsrc += KV_CHBYTES;
  };
  float mrun = -3.0e38f;
  stage(0);
  for (int ch = 0; ch < NCH / 2; ++ch) {
    WAITV(0);
    BARRIER();
    if (ch + 1 < NCH / 2) stage((ch + 1) & 1);
    const char* base = lds + (ch & 1) * (2 * KV_CHBYTES);
    bf16x8 k0 = *(const bf16x8*)(base + koff0);
    bf16x8 k1 = *(const bf16x8*)(base + koff1);
    bf16x8 v0 = *(const bf16x8*)(base + voff);
    bf16x8 v1 = *(const bf16x8*)(base + voff + KV_VSTR);
    if (!SAFE && ch == 0) {
      f32x16 z = {};
      f32x16 s = MFMA32(k0, qf0, z);
      s = MFMA32(k1, qf1, s);
      float m = s[0];
#pragma unroll
      for (int i = 1; i < 16; ++i) m = fmaxf(m, s[i]);
      m = fmaxf(m, __shfl_xor(m, 32));
      const u16 mb = f2bf(m);
      shift = bf2f(mb);
      if (hh == 0) qf1[4] = (short)(mb ^ 0x8000u);
    }
#pragma unroll SAFE ? 1 : 4
    for (int t = 0; t < KVCH / 32; ++t) {
      f32x16 z = {};
      f32x16 s = MFMA32(k0, qf0, z);
      s = MFMA32(k1, qf1, s);
      bf16x8 cv0 = v0, cv1 = v1;
      if (t + 1 < KVCH / 32) {
        k0 = *(const bf16x8*)(base + (t + 1) * 32 * 48 + koff0);
        k1 = *(const bf16x8*)(base + (t + 1) * 32 * 48 + koff1);
        v0 = *(const bf16x8*)(base + voff + (2 * t + 2) * KV_VSTR);
        v1 = *(const bf16x8*)(base + voff + (2 * t + 3) * KV_VSTR);
      }
      if (SAFE) {
        float m = s[0];
#pragma unroll
        for (int i = 1; i < 16; ++i) m = fmaxf(m, s[i]);
        m = fmaxf(m, __shfl_xor(m, 32));
        const float mn = fmaxf(mrun, m);
        const float alpha = __builtin_amdgcn_exp2f(mrun - mn);
        mrun = mn;
#pragma unroll
        for (int i = 0; i < 16; ++i) { o[i] *= alpha; s[i] -= mn; }
      }
      u32x4 p0, p1;
#pragma unroll
      for (int i = 0; i < 4; ++i) {
        p0[i] = pk2(__builtin_amdgcn_exp2f(s[2 * i]), __builtin_amdgcn_exp2f(s[2 * i + 1]));
        p1[i] = pk2(__builtin_amdgcn_exp2f(s[8 + 2 * i]), __builtin_amdgcn_exp2f(s[8 + 2 * i + 1]));
      }
      o = MFMA32(cv0, __builtin_bit_cast(bf16x8, p0), o);
      o = MFMA32(cv1, __builtin_bit_cast(bf16x8, p1), o);
    }
  }
  if (SAFE) shift = mrun;
}

__global__ __launch_bounds__(512, 5) void stage2_kernel(const u16* __restrict__ w1, const u16* __restrict__ b1,
                                                        char* __restrict__ ws) {
  extern __shared__ __attribute__((aligned(16))) char lds[];
  const int tid = threadIdx.x, lane = tid & 63;
  const int wave = __builtin_amdgcn_readfirstlane(tid >> 6);
  if (blockIdx.x < G1_WGS) {
    if (wave >= 4) return;
    const int bid = blockIdx.x;
    const int xcd = bid & 7, j = bid >> 3;
    const int tf = xcd * 3 + j / 10, td = j % 10;
    const int d0 = td * 64, f0 = tf * 64;
    const u16* h = (const u16*)(ws + WS_H);
    u16* u = (u16*)(ws + WS_U);
    f32x4 acc[5] = {};
    gemm_core<false, 4, G1_NST>(acc, h + d0, DM, w1 + f0, FF, SEQ / 64, (td * 3 + (j / 10)) & 31, lds, wave, lane);
    const int f = f0 + 16 * wave + (lane & 15);
    const float bias = bf2f(b1[f]);
#pragma unroll
    for (int ct = 0; ct < 4; ++ct) {
      float r[4];
#pragma unroll
      for (int i = 0; i < 4; ++i) {
        const float t = acc[ct][i] + bias;
        const float z = 0.7978845608028654f * (t + 0.044715f * t * t * t);
        r[i] = t / (1.f + __expf(-2.f * z));
      }
      u32x2 v = {pk2(r[0], r[1]), pk2(r[2], r[3])};
      *(u32x2*)(u + (size_t)f * DM + d0 + 16 * ct + 4 * (lane >> 4)) = v;
    }
    return;
  }
  const int half = wave >> 2;
  const int s = (blockIdx.x - G1_WGS) * 4 + (wave & 3);
  f32x16 o = {};
  float shift = 0.f;
  volatile unsigned* flag = (volatile unsigned*)(lds + S2_FLAG);
  if (tid == 0) *flag = 0u;
  attn_pass<false>(ws, lds, s, o, shift, wave, lane);
  int lane2 = lane;
  asm volatile("" : "+v"(lane2));
  const int hd = lane2 & 31, hh = lane2 >> 5;
  {
    const float l = __shfl(o[8], hd + 32);
    const bool bad = !(l > 8.67e-19f && l < 1.15e18f);
    if (bad) *flag = 1u;
    __syncthreads();
    if (__builtin_amdgcn_readfirstlane(*flag) != 0u) {
      o = f32x16{};
      attn_pass<true>(ws, lds, s, o, shift, wave, lane2);
    }
  }
  __syncthreads();
  float* mg = (float*)lds + (wave & 3) * 13 * 64;
  if (half == 1) {
#pragma unroll
    for (int i = 0; i < 12; ++i) mg[i * 64 + lane2] = o[i];
    mg[12 * 64 + lane2] = shift;
  }
  __syncthreads();
  if (half == 0) {
    const float sh1 = mg[12 * 64 + lane2];
    const float mm = fmaxf(shift, sh1);
    const float f0 = __builtin_amdgcn_exp2f(shift - mm), f1 = __builtin_amdgcn_exp2f(sh1 - mm);
#pragma unroll
    for (int i = 0; i < 12; ++i) o[i] = o[i] * f0 + mg[i * 64 + lane2] * f1;
    const float l = __shfl(o[8], hd + 32);
    const float inv = 1.f / l;
    u16* op = (u16*)(ws + WS_O) + (size_t)s * DM + hd * DH + 4 * hh;
#pragma unroll
    for (int g = 0; g < 3; ++g) {
      if (g < 2 || hh == 0) {
        u32x2 v = {pk2(o[4 * g] * inv, o[4 * g + 1] * inv), pk2(o[4 * g + 2] * inv, o[4 * g + 3] * inv)};
        *(u32x2*)(op + 8 * g) = v;
      }
    }
  }
}

constexpr int S3_NST = 5;
constexpr int S3_LDS = S3_NST * GSTG;
__global__ __launch_bounds__(256) void stage3_kernel(const u16* __restrict__ wo, const u16* __restrict__ w2,
                                                     const u16* __restrict__ b2, const char* __restrict__ ws,
                                                     u16* __restrict__ out) {
  extern __shared__ __attribute__((aligned(16))) char lds[];
  const int tid = threadIdx.x, lane = tid & 63;
  const int wave = __builtin_amdgcn_readfirstlane(tid >> 6);
  const int bid = blockIdx.x;
  const int xcd = bid & 7, j = bid >> 3;
  const int ts = xcd * 4 + (j >> 3), td = j & 7;
  const int d0 = td * 80, s0 = ts * 64;
  const u16* o = (const u16*)(ws + WS_O);
  const u16* u = (const u16*)(ws + WS_U);
  const float* gate = (const float*)(ws + WS_G);
  f32x4 acca[5] = {};
  f32x4 accb[5] = {};
  {
    GemmSeg sb, sa;
    seg_init<false>(sb, u + d0, DM, w2 + s0, SEQ, FF / 64, td * 3, tid);
    seg_init<true>(sa, wo + d0, DM, o + (size_t)s0 * DM, DM, DM / 64, (td + 3 * (j >> 3)) % 10, tid);
    const FragAddr f = frag_addr(wave, lane);
    const unsigned lbase = lds_addr(lds) + wave * 1024;
    constexpr int NKB = FF / 64, NKA = DM / 64, NT = NKB + NKA;
#pragma unroll
    for (int i = 0; i < S3_NST - 1; ++i) seg_stage<5>(sb, lbase + i * GSTG, wave);
    int buf = 0;
    for (int kt = 0; kt < NT; ++kt) {
      const int rem = NT - 1 - kt;
      wait_stages<5>(rem < S3_NST - 2 ? rem : S3_NST - 2, wave);
      BARRIER();
      const int nx = kt + S3_NST - 1;
      const unsigned nb = lbase + (buf == 0 ? S3_NST - 1 : buf - 1) * GSTG;
      if (nx < NKB) seg_stage<5>(sb, nb, wave);
      else if (nx < NT) seg_stage<5>(sa, nb, wave);
      if (kt < NKB) gemm_step<false, 5>(accb, lds + buf * GSTG, f);
      else gemm_step<true, 5>(acca, lds + buf * GSTG, f);
      buf = buf == S3_NST - 1 ? 0 : buf + 1;
    }
  }
  const int s = s0 + 16 * wave + (lane & 15);
  const float g = gate[s], bias = bf2f(b2[s]);
#pragma unroll
  for (int ct = 0; ct < 5; ++ct) {
    float r[4];
#pragma unroll
    for (int i = 0; i < 4; ++i) r[i] = g * acca[ct][i] + (1.f - g) * (accb[ct][i] + bias);
    u32x2 v = {pk2(r[0], r[1]), pk2(r[2], r[3])};
    *(u32x2*)(out + (size_t)s * DM + d0 + 16 * ct + 4 * (lane >> 4)) = v;
  }
}

extern "C" void kernel_launch(void* const* d_in, const int* in_sizes, int n_in, void* d_out, int out_size, void* d_ws,
                              size_t ws_size, hipStream_t stream) {
  const u16* x = (const u16*)d_in[0];
  const u16* wq = (const u16*)d_in[1];
  const u16* wk = (const u16*)d_in[2];
  const u16* wv = (const u16*)d_in[3];
  const u16* wo = (const u16*)d_in[4];
  const u16* w1 = (const u16*)d_in[6];
  const u16* b1 = (const u16*)d_in[7];
  const u16* w2 = (const u16*)d_in[8];
  const u16* b2 = (const u16*)d_in[9];
  const u16* gw = (const u16*)d_in[10];
  char* ws = (char*)d_ws;
  (void)hipFuncSetAttribute((const void*)stage1_kernel, hipFuncAttributeMaxDynamicSharedMemorySize, S1_LDS);
  (void)hipFuncSetAttribute((const void*)stage3_kernel, hipFuncAttributeMaxDynamicSharedMemorySize, S3_LDS);
  stage1_kernel<<<S1_QT + S1_KVT + S1_LNT, 256, S1_LDS, stream>>>(x, wq, wk, wv, gw, ws);
  stage2_kernel<<<G1_WGS + AT_WGS, 512, S2_LDS, stream>>>(w1, b1, ws);
  stage3_kernel<<<256, 256, S3_LDS, stream>>>(wo, w2, b2, ws, (u16*)d_out);
}
```

```cpp
#include <hip/hip_runtime.h>
#include <hip/hip_bf16.h>
#include <stdint.h>

typedef short bf16x8 __attribute__((ext_vector_type(8)));
typedef short bf16x4 __attribute__((ext_vector_type(4)));
typedef float f32x16 __attribute__((ext_vector_type(16)));
typedef float f32x4 __attribute__((ext_vector_type(4)));
typedef float f32x2 __attribute__((ext_vector_type(2)));
typedef __bf16 bfx2 __attribute__((ext_vector_type(2)));
typedef unsigned u32x4 __attribute__((ext_vector_type(4)));
typedef unsigned u32x2 __attribute__((ext_vector_type(2)));
typedef unsigned short u16;
typedef __attribute__((address_space(3))) bf16x4* lds_b4p;

#define DI __device__ __forceinline__
#define MFMA32(a, b, c) __builtin_amdgcn_mfma_f32_32x32x16_bf16((a), (b), (c), 0, 0, 0)
#define MFMA32K8(a, b, c) __builtin_amdgcn_mfma_f32_32x32x8bf16_1k((a), (b), (c), 0, 0, 0)
#define MFMA16(a, b, c) __builtin_amdgcn_mfma_f32_16x16x32_bf16((a), (b), (c), 0, 0, 0)

constexpr int SEQ = 2048, DM = 640, NH = 32, DH = 20, FF = 1536;
constexpr int KVCH = 128;
constexpr int KV_KBYTES = KVCH * 48;
constexpr int KV_VSTR = 672;
constexpr int KV_CHBYTES = 12288;
constexpr int NCH = SEQ / KVCH;

constexpr size_t WS_Q = 0;
constexpr size_t WS_H = WS_Q + (size_t)SEQ * DM * 2;
constexpr size_t WS_O = WS_H + (size_t)SEQ * DM * 2;
constexpr size_t WS_U = WS_O + (size_t)SEQ * DM * 2;
constexpr size_t WS_KV = WS_U + (size_t)FF * DM * 2;
constexpr size_t WS_G = WS_KV + (size_t)NCH * KV_CHBYTES;

DI unsigned pk2(float lo, float hi) {
  f32x2 v = {lo, hi};
  bfx2 b = __builtin_convertvector(v, bfx2);
  return __builtin_bit_cast(unsigned, b);
}
DI float bf2f(u16 v) { return __uint_as_float(((unsigned)v) << 16); }
DI u16 f2bf(float f) { return (u16)(pk2(f, 0.f) & 0xffffu); }

DI void glds16(const void* g, unsigned ldsaddr) {
  unsigned keep;
  asm volatile("s_mov_b32 %0, m0\n\ts_mov_b32 m0, %2\n\ts_nop 0\n\tglobal_load_lds_dwordx4 %1, off\n\ts_mov_b32 m0, %0"
               : "=&s"(keep)
               : "v"(g), "s"(ldsaddr)
               : "memory");
}
DI unsigned lds_addr(const void* p) { return (unsigned)(size_t)p; }

#define WAITV(n) asm volatile("s_waitcnt vmcnt(" #n ")" ::: "memory")
#define BARRIER() do { asm volatile("s_waitcnt lgkmcnt(0)" ::: "memory"); __builtin_amdgcn_s_barrier(); asm volatile("" ::: "memory"); } while (0)

DI bf16x8 tr_pair(const char* p0, const char* p1) {
  bf16x4 lo = __builtin_amdgcn_ds_read_tr16_b64_v4i16((lds_b4p)(p0));
  bf16x4 hi = __builtin_amdgcn_ds_read_tr16_b64_v4i16((lds_b4p)(p1));
  return __builtin_shufflevector(lo, hi, 0, 1, 2, 3, 4, 5, 6, 7);
}

constexpr int GSTG = 18432;
template <int NCT> constexpr int gboff() { return NCT == 5 ? 10240 : 8192; }
template <int NCT> constexpr int gstg() { return gboff<NCT>() + 8192; }
DI int colswz(int r) { return (((r >> 1) & 1) | (((r >> 3) & 1) << 1)) << 1; }

struct GemmSeg {
  const u16* src[5];
  size_t astep, bstep;
  int nk, kcur;
};
template <bool BROW>
DI void seg_init(GemmSeg& sg, const u16* A, int lda, const u16* B, int ldb, int nk, int k0, int tid) {
  sg.astep = (size_t)64 * lda;
  sg.bstep = BROW ? (size_t)64 : (size_t)64 * ldb;
  sg.nk = nk;
  sg.kcur = k0;
  A += (size_t)k0 * sg.astep;
  B += (size_t)k0 * sg.bstep;
#pragma unroll
  for (int i = 0; i < 2; ++i) {
    const int c = tid + 256 * i, r = c >> 3, ch = (c & 7) ^ colswz(r);
    sg.src[i] = A + (size_t)r * lda + ch * 8;
    if (BROW) sg.src[2 + i] = B + (size_t)r * ldb + ((c & 7) ^ ((r >> 1) & 7)) * 8;
    else sg.src[2 + i] = B + (size_t)r * ldb + ch * 8;
  }
  const int pos = (tid & 127) >> 1, r = pos ^ (((pos >> 3) & 1) << 2);
  sg.src[4] = A + (size_t)r * lda + 64 + (tid & 1) * 8;
}
template <int NCT>
DI void seg_stage(GemmSeg& sg, unsigned b  , int wave) {
  glds16(sg.src[0], b);
  glds16(sg.src[1], b + 4096);
  glds16(sg.src[2], b + gboff<NCT>());
  glds16(sg.src[3], b + gboff<NCT>() + 4096);
  if (NCT == 5 && wave < 2) glds16(sg.src[4], b + 8192);
  sg.kcur += 1;
  if (sg.kcur == sg.nk) {
    sg.kcur = 0;
    const size_t ab = sg.astep * (sg.nk - 1), bb = sg.bstep * (sg.nk - 1);
    sg.src[0] -= ab; sg.src[1] -= ab; sg.src[4] -= ab;
    sg.src[2] -= bb; sg.src[3] -= bb;
  } else {
    sg.src[0] += sg.astep; sg.src[1] += sg.astep; sg.src[4] += sg.astep;
    sg.src[2] += sg.bstep; sg.src[3] += sg.bstep;
  }
}
struct FragAddr { int aoff, asw, a2off, boffc, boffr, bsw, g; };
DI FragAddr frag_addr(int wave, int lane) {
  FragAddr f;
  const int g = lane >> 4, q = (lane & 15) >> 2, p = lane & 3;
  const int kr = 8 * g + q;
  f.g = g;
  f.aoff = kr * 128 + 8 * p;
  f.asw = colswz(kr);
  f.a2off = 8192 + (kr ^ (((kr >> 3) & 1) << 2)) * 32 + 8 * p;
  const int row = 16 * wave + (lane & 15);
  f.boffr = row * 128;
  f.boffc = kr * 128 + (((2 * wave) ^ f.asw) << 4) + 8 * p;
  f.bsw = (row >> 1) & 7;
  return f;
}
template <bool BROW, int NCT>
DI void gemm_step(f32x4 (&acc)[5], const char* base, const FragAddr& f) {
#pragma unroll
  for (int ks = 0; ks < 2; ++ks) {
    bf16x8 fa[5], fb;
#pragma unroll
    for (int ct = 0; ct < 4; ++ct) {
      const char* a = base + ks * 4096 + f.aoff + (((2 * ct) ^ f.asw) << 4);
      fa[ct] = tr_pair(a, a + 512);
    }
    if (NCT == 5) fa[4] = tr_pair(base + ks * 1024 + f.a2off, base + ks * 1024 + (f.a2off ^ 128));
    const char* bb = base + gboff<NCT>();
    if (BROW) fb = *(const bf16x8*)(bb + f.boffr + (((4 * ks + f.g) ^ f.bsw) << 4));
    else fb = tr_pair(bb + ks * 4096 + f.boffc, bb + ks * 4096 + f.boffc + 512);
#pragma unroll
    for (int ct = 0; ct < NCT; ++ct) acc[ct] = MFMA16(fa[ct], fb, acc[ct]);
  }
}
template <int NCT>
DI void wait_stages(int n, int wave) {
  if (NCT == 5 && wave < 2) {
    if (n >= 4) WAITV(20); else if (n == 3) WAITV(15); else if (n == 2) WAITV(10); else if (n == 1) WAITV(5); else WAITV(0);
  } else {
    if (n >= 4) WAITV(16); else if (n == 3) WAITV(12); else if (n == 2) WAITV(8); else if (n == 1) WAITV(4); else WAITV(0);
  }
}
template <int NCT, int NST>
DI void gemm_loader(GemmSeg& s0, int n0, GemmSeg& s1, int n1, char* lds, int lw) {
  const unsigned lbase = lds_addr(lds) + lw * 1024;
  const int nt = n0 + n1;
#pragma unroll
  for (int i = 0; i < NST - 1; ++i) seg_stage<NCT>(s0, lbase + i * gstg<NCT>(), lw);
  int nb = NST - 1;
  for (int kt = 0; kt < nt; ++kt) {
    const int rem = nt - 1 - kt;
    wait_stages<NCT>(rem < NST - 2 ? rem : NST - 2, lw);
    __builtin_amdgcn_s_barrier();
    const int nx = kt + NST - 1;
    if (nx < n0) seg_stage<NCT>(s0, lbase + nb * gstg<NCT>(), lw);
    else if (nx < nt) seg_stage<NCT>(s1, lbase + nb * gstg<NCT>(), lw);
    nb = nb == NST - 1 ? 0 : nb + 1;
  }
}
template <bool BROW, int NCT, int NST>
DI void gemm_consume(f32x4 (&acc)[5], int nk, int& buf, char* lds, const FragAddr& f) {
  for (int kt = 0; kt < nk; ++kt) {
    BARRIER();
    gemm_step<BROW, NCT>(acc, lds + buf * gstg<NCT>(), f);
    buf = buf == NST - 1 ? 0 : buf + 1;
  }
}

constexpr int S1_NST = 4;
constexpr int S1_QT = 256;
constexpr int S1_KVT = SEQ / 64;
constexpr int S1_LNT = SEQ / 16;
constexpr int S1_LDS = 640 * 80 + 1024 + 3 * 8192;

DI void stage_row32(const u16* src, int ld, unsigned img, int wave, int lane) {
#pragma unroll
  for (int i = 0; i < 2; ++i) {
    const int row = (wave * 2 + i) * 8 + (lane >> 3);
    const int c = (lane & 7) ^ ((row >> 1) & 7);
    glds16(src + (size_t)row * ld + c * 8, img + (wave * 2 + i) * 1024);
  }
}

__global__ __launch_bounds__(512) void stage1_kernel(const u16* __restrict__ x, const u16* __restrict__ wq,
                                                     const u16* __restrict__ wk, const u16* __restrict__ wv,
                                                     const u16* __restrict__ gw, char* __restrict__ ws) {
  extern __shared__ __attribute__((aligned(16))) char lds[];
  const int tid = threadIdx.x, lane = tid & 63;
  const int wave = __builtin_amdgcn_readfirstlane(tid >> 6);
  int bid = blockIdx.x;
  if (bid < S1_QT) {
    const int xcd = bid & 7, j = bid >> 3;
    const int ts = xcd * 4 + (j >> 3), tn = j & 7;
    const int n0 = tn * 80, s0 = ts * 64;
    if (wave >= 4) {
      GemmSeg sg;
      seg_init<true>(sg, wq + n0, DM, x + (size_t)s0 * DM, DM, DM / 64, (tn + 3 * (j >> 3)) % 10, tid - 256);
      gemm_loader<5, S1_NST>(sg, DM / 64, sg, 0, lds, wave - 4);
      return;
    }
    f32x4 acc[5] = {};
    {
      const FragAddr f = frag_addr(wave, lane);
      int buf = 0;
      gemm_consume<true, 5, S1_NST>(acc, DM / 64, buf, lds, f);
    }
    const float qs = 1.4426950408889634f * 0.22360679774997896f;
    u16* q = (u16*)(ws + WS_Q);
    const int s = s0 + 16 * wave + (lane & 15);
#pragma unroll
    for (int ct = 0; ct < 5; ++ct) {
      u32x2 v = {pk2(acc[ct][0] * qs, acc[ct][1] * qs), pk2(acc[ct][2] * qs, acc[ct][3] * qs)};
      *(u32x2*)(q + (size_t)s * DM + n0 + 16 * ct + 4 * (lane >> 4)) = v;
    }
    return;
  }
  bid -= S1_QT;
  if (wave >= 4) return;
  if (bid < S1_KVT) {
    const int wm = wave >> 1, wn = wave & 1;
    const int s0 = bid * 64;
    char* wimg = lds;
    char* aimg = lds + 640 * 80 + 1024;
    for (int i = wave; i < 50; i += 4) {
      const char* src = i < 25 ? ((const char*)wk + i * 1024) : ((const char*)wv + (i - 25) * 1024);
      glds16(src + lane * 16, lds_addr(wimg) + i * 1024);
    }
    stage_row32(x + (size_t)s0 * DM, DM, lds_addr(aimg), wave, lane);
    stage_row32(x + (size_t)s0 * DM + 64, DM, lds_addr(aimg) + 8192, wave, lane);
    f32x16 acc = {};
    int buf = 0;
    const int i16 = lane & 15, q4 = i16 >> 2, p4 = i16 & 3, hh = lane >> 5, g1 = (lane >> 4) & 1;
    for (int kt = 0; kt < 10; ++kt) {
      if (kt + 1 < 10) WAITV(2); else WAITV(0);
      BARRIER();
      if (kt + 2 < 10)
        stage_row32(x + (size_t)s0 * DM + (kt + 2) * 64, DM, lds_addr(aimg) + (buf == 0 ? 2 : buf - 1) * 8192, wave, lane);
#pragma unroll
      for (int kk = 0; kk < 4; ++kk) {
        const int krow = kt * 64 + 16 * kk + 8 * hh + q4;
        const char* ap = wimg + wm * 25600 + krow * 40 + (16 * g1 + 4 * p4) * 2;
        bf16x8 fa = tr_pair(ap, ap + 4 * 40);
        const int row = 32 * wn + (lane & 31);
        const int c = 2 * kk + hh;
        bf16x8 fb = *(const bf16x8*)(aimg + buf * 8192 + row * 128 + ((c ^ ((row >> 1) & 7)) << 4));
        acc = MFMA32(fa, fb, acc);
      }
      buf = buf == 2 ? 0 : buf + 1;
    }
    const int s = s0 + 32 * wn + (lane & 31);
    char* kvp = ws + WS_KV + (size_t)(s / KVCH) * KV_CHBYTES;
    const int key = s % KVCH;
    const int kb = key >> 4, r = key & 15, vh = (r >> 2) & 1, vj = (r & 3) + 4 * (r >> 3);
    char* vbase = kvp + KV_KBYTES + kb * KV_VSTR + vh * 336 + vj * 2;
#pragma unroll
    for (int g = 0; g < 4; ++g) {
      const int c0 = 8 * g + 4 * hh;
      if (c0 < 20) {
        if (wm == 0) {
          u32x2 v = {pk2(acc[4 * g], acc[4 * g + 1]), pk2(acc[4 * g + 2], acc[4 * g + 3])};
          *(u32x2*)(kvp + key * 48 + c0 * 2) = v;
        } else {
#pragma unroll
          for (int j = 0; j < 4; ++j) *(u16*)(vbase + (c0 + j) * 16) = f2bf(acc[4 * g + j]);
        }
      }
    }
    if (wm == 0 && hh == 0) {
      u32x2 v = {0x00003f80u, 0u};
      *(u32x2*)(kvp + key * 48 + 40) = v;
      *(u16*)(vbase + 20 * 16) = 0x3f80;
    }
    return;
  }
  bid -= S1_KVT;
  {
    u16* h = (u16*)(ws + WS_H);
    float* gate = (float*)(ws + WS_G);
    float gwv[10];
#pragma unroll
    for (int i = 0; i < 5; ++i) {
      const unsigned u = *(const unsigned*)(gw + (lane + 64 * i) * 2);
      gwv[2 * i] = __uint_as_float(u << 16);
      gwv[2 * i + 1] = __uint_as_float(u & 0xffff0000u);
    }
#pragma unroll
    for (int rr = 0; rr < 4; ++rr) {
      const int s = bid * 16 + wave * 4 + rr;
      float v[10];
      float sum = 0.f, dot = 0.f;
#pragma unroll
      for (int i = 0; i < 5; ++i) {
        const unsigned u = *(const unsigned*)(x + (size_t)s * DM + (lane + 64 * i) * 2);
        v[2 * i] = __uint_as_float(u << 16);
        v[2 * i + 1] = __uint_as_float(u & 0xffff0000u);
        sum += v[2 * i] + v[2 * i + 1];
        dot += v[2 * i] * gwv[2 * i] + v[2 * i + 1] * gwv[2 * i + 1];
      }
#pragma unroll
      for (int o = 32; o >= 1; o >>= 1) { sum += __shfl_xor(sum, o); dot += __shfl_xor(dot, o); }
      const float mean = sum * (1.f / DM);
      float var = 0.f;
#pragma unroll
      for (int i = 0; i < 10; ++i) { const float d = v[i] - mean; var += d * d; }
#pragma unroll
      for (int o = 32; o >= 1; o >>= 1) var += __shfl_xor(var, o);
      const float rstd = 1.f / sqrtf(var * (1.f / DM) + 1e-5f);
#pragma unroll
      for (int i = 0; i < 5; ++i)
        *(unsigned*)(h + (size_t)s * DM + (lane + 64 * i) * 2) = pk2((v[2 * i] - mean) * rstd, (v[2 * i + 1] - mean) * rstd);
      if (lane == 0) gate[s] = 1.f / (1.f + __expf(-dot));
    }
  }
}

constexpr int G1_WGS = 240;
constexpr int AT_WGS = SEQ / 4;
constexpr int G1_NST = 3;
constexpr int S2_FLAG = 4 * KV_CHBYTES;
constexpr int S2_LDS = S2_FLAG + 64;

template <bool SAFE>
DI void attn_pass(const char* __restrict__ ws, char* lds, int srow, f32x16& o, float& shift, int wave, int lane) {
  const int hd = lane & 31, hh = lane >> 5;
  const int half = wave >> 2;
  const char* kvg = ws + WS_KV;
  bf16x8 qf0;
  bf16x4 qf1;
  {
    const u16* q = (const u16*)(ws + WS_Q) + (size_t)srow * DM + hd * DH;
    u32x2 a = *(const u32x2*)(q + 8 * hh), b = *(const u32x2*)(q + 8 * hh + 4), c = *(const u32x2*)(q + 16);
    u32x4 t0 = {a[0], a[1], b[0], b[1]};
    u32x2 t1 = {hh == 0 ? c[0] : 0u, hh == 0 ? c[1] : 0u};
    qf0 = __builtin_bit_cast(bf16x8, t0);
    qf1 = __builtin_bit_cast(bf16x4, t1);
  }
  const int koff0 = half * KV_CHBYTES + hd * 48 + 16 * hh, koff1 = half * KV_CHBYTES + hd * 48 + 32 + 8 * hh;
  const int voff = half * KV_CHBYTES + KV_KBYTES + hh * 336 + (hd < 20 ? hd : 20) * 16;
  const char* gsrc = kvg + (size_t)(half * (NCH / 2)) * KV_CHBYTES + (wave & 3) * 3072 + lane * 16;
  const unsigned ldst = lds_addr(lds) + wave * 3072;
  auto stage = [&](int buf) {
#pragma unroll
    for (int i = 0; i < 3; ++i) glds16(gsrc + i * 1024, ldst + buf * (2 * KV_CHBYTES) + i * 1024);
    gsrc += KV_CHBYTES;
  };
  float mrun = -3.0e38f;
  stage(0);
  for (int ch = 0; ch < NCH / 2; ++ch) {
    WAITV(0);
    BARRIER();
    if (ch + 1 < NCH / 2) stage((ch + 1) & 1);
    const char* base = lds + (ch & 1) * (2 * KV_CHBYTES);
    bf16x8 k0 = *(const bf16x8*)(base + koff0);
    bf16x4 k1 = *(const bf16x4*)(base + koff1);
    bf16x8 v0 = *(const bf16x8*)(base + voff);
    bf16x8 v1 = *(const bf16x8*)(base + voff + KV_VSTR);
    if (!SAFE && ch == 0) {
      f32x16 z = {};
      f32x16 s = MFMA32(k0, qf0, z);
      s = MFMA32K8(k1, qf1, s);
      float m = s[0];
#pragma unroll
      for (int i = 1; i < 16; ++i) m = fmaxf(m, s[i]);
      m = fmaxf(m, __shfl_xor(m, 32));
      const u16 mb = f2bf(m);
      shift = bf2f(mb);
      if (hh == 1) qf1[0] = (short)(mb ^ 0x8000u);
    }
#pragma unroll SAFE ? 1 : 4
    for (int t = 0; t < KVCH / 32; ++t) {
      f32x16 z = {};
      f32x16 s = MFMA32(k0, qf0, z);
      s = MFMA32K8(k1, qf1, s);
      bf16x8 cv0 = v0, cv1 = v1;
      if (t + 1 < KVCH / 32) {
        k0 = *(const bf16x8*)(base + (t + 1) * 32 * 48 + koff0);
        k1 = *(const bf16x4*)(base + (t + 1) * 32 * 48 + koff1);
        v0 = *(const bf16x8*)(base + voff + (2 * t + 2) * KV_VSTR);
        v1 = *(const bf16x8*)(base + voff + (2 * t + 3) * KV_VSTR);
      }
      if (SAFE) {
        float m = s[0];
#pragma unroll
        for (int i = 1; i < 16; ++i) m = fmaxf(m, s[i]);
        m = fmaxf(m, __shfl_xor(m, 32));
        const float mn = fmaxf(mrun, m);
        const float alpha = __builtin_amdgcn_exp2f(mrun - mn);
        mrun = mn;
#pragma unroll
        for (int i = 0; i < 16; ++i) { o[i] *= alpha; s[i] -= mn; }
      }
      u32x4 p0, p1;
#pragma unroll
      for (int i = 0; i < 4; ++i) {
        p0[i] = pk2(__builtin_amdgcn_exp2f(s[2 * i]), __builtin_amdgcn_exp2f(s[2 * i + 1]));
        p1[i] = pk2(__builtin_amdgcn_exp2f(s[8 + 2 * i]), __builtin_amdgcn_exp2f(s[8 + 2 * i + 1]));
      }
      o = MFMA32(cv0, __builtin_bit_cast(bf16x8, p0), o);
      o = MFMA32(cv1, __builtin_bit_cast(bf16x8, p1), o);
    }
  }
  if (SAFE) shift = mrun;
}

__global__ __launch_bounds__(512, 6) void stage2_kernel(const u16* __restrict__ w1, const u16* __restrict__ b1,
                                                        char* __restrict__ ws) {
  extern __shared__ __attribute__((aligned(16))) char lds[];
  const int tid = threadIdx.x, lane = tid & 63;
  const int wave = __builtin_amdgcn_readfirstlane(tid >> 6);
  if (blockIdx.x < G1_WGS) {
    const int bid = blockIdx.x;
    const int xcd = bid & 7, j = bid >> 3;
    const int tf = xcd * 3 + j / 10, td = j % 10;
    const int d0 = td * 64, f0 = tf * 64;
    const u16* h = (const u16*)(ws + WS_H);
    u16* u = (u16*)(ws + WS_U);
    if (wave >= 4) {
      GemmSeg sg;
      seg_init<false>(sg, h + d0, DM, w1 + f0, FF, SEQ / 64, (td * 3 + (j / 10)) & 31, tid - 256);
      gemm_loader<4, G1_NST>(sg, SEQ / 64, sg, 0, lds, wave - 4);
      return;
    }
    f32x4 acc[5] = {};
    {
      const FragAddr f = frag_addr(wave, lane);
      int buf = 0;
      gemm_consume<false, 4, G1_NST>(acc, SEQ / 64, buf, lds, f);
    }
    const int f = f0 + 16 * wave + (lane & 15);
    const float bias = bf2f(b1[f]);
#pragma unroll
    for (int ct = 0; ct < 4; ++ct) {
      float r[4];
#pragma unroll
      for (int i = 0; i < 4; ++i) {
        const float t = acc[ct][i] + bias;
        const float z = 0.7978845608028654f * (t + 0.044715f * t * t * t);
        r[i] = t / (1.f + __expf(-2.f * z));
      }
      u32x2 v = {pk2(r[0], r[1]), pk2(r[2], r[3])};
      *(u32x2*)(u + (size_t)f * DM + d0 + 16 * ct + 4 * (lane >> 4)) = v;
    }
    return;
  }
  const int half = wave >> 2;
  const int s = (blockIdx.x - G1_WGS) * 4 + (wave & 3);
  f32x16 o = {};
  float shift = 0.f;
  volatile unsigned* flag = (volatile unsigned*)(lds + S2_FLAG);
  if (tid == 0) *flag = 0u;
  attn_pass<false>(ws, lds, s, o, shift, wave, lane);
  int lane2 = lane;
  asm volatile("" : "+v"(lane2));
  const int hd = lane2 & 31, hh = lane2 >> 5;
  {
    const float l = __shfl(o[8], hd + 32);
    const bool bad = !(l > 8.67e-19f && l < 1.15e18f);
    if (bad) *flag = 1u;
    __syncthreads();
    if (__builtin_amdgcn_readfirstlane(*flag) != 0u) {
      o = f32x16{};
      attn_pass<true>(ws, lds, s, o, shift, wave, lane2);
    }
  }
  __syncthreads();
  float* mg = (float*)lds + (wave & 3) * 13 * 64;
  if (half == 1) {
#pragma unroll
    for (int i = 0; i < 12; ++i) mg[i * 64 + lane2] = o[i];
    mg[12 * 64 + lane2] = shift;
  }
  __syncthreads();
  if (half == 0) {
    const float sh1 = mg[12 * 64 + lane2];
    const float mm = fmaxf(shift, sh1);
    const float f0 = __builtin_amdgcn_exp2f(shift - mm), f1 = __builtin_amdgcn_exp2f(sh1 - mm);
#pragma unroll
    for (int i = 0; i < 12; ++i) o[i] = o[i] * f0 + mg[i * 64 + lane2] * f1;
    const float l = __shfl(o[8], hd + 32);
    const float inv = 1.f / l;
    u16* op = (u16*)(ws + WS_O) + (size_t)s * DM + hd * DH + 4 * hh;
#pragma unroll
    for (int g = 0; g < 3; ++g) {
      if (g < 2 || hh == 0) {
        u32x2 v = {pk2(o[4 * g] * inv, o[4 * g + 1] * inv), pk2(o[4 * g + 2] * inv, o[4 * g + 3] * inv)};
        *(u32x2*)(op + 8 * g) = v;
      }
    }
  }
}

constexpr int S3_NST = 5;
constexpr int S3_LDS = S3_NST * GSTG;
__global__ __launch_bounds__(512) void stage3_kernel(const u16* __restrict__ wo, const u16* __restrict__ w2,
                                                     const u16* __restrict__ b2, const char* __restrict__ ws,
                                                     u16* __restrict__ out) {
  extern __shared__ __attribute__((aligned(16))) char lds[];
  const int tid = threadIdx.x, lane = tid & 63;
  const int wave = __builtin_amdgcn_readfirstlane(tid >> 6);
  const int bid = blockIdx.x;
  const int xcd = bid & 7, j = bid >> 3;
  const int ts = xcd * 4 + (j >> 3), td = j & 7;
  const int d0 = td * 80, s0 = ts * 64;
  const u16* o = (const u16*)(ws + WS_O);
  const u16* u = (const u16*)(ws + WS_U);
  const float* gate = (const float*)(ws + WS_G);
  if (wave >= 4) {
    GemmSeg sb, sa;
    seg_init<false>(sb, u + d0, DM, w2 + s0, SEQ, FF / 64, td * 3, tid - 256);
    seg_init<true>(sa, wo + d0, DM, o + (size_t)s0 * DM, DM, DM / 64, (td + 3 * (j >> 3)) % 10, tid - 256);
    gemm_loader<5, S3_NST>(sb, FF / 64, sa, DM / 64, lds, wave - 4);
    return;
  }
  f32x4 acca[5] = {};
  f32x4 accb[5] = {};
  {
    const FragAddr f = frag_addr(wave, lane);
    int buf = 0;
    gemm_consume<false, 5, S3_NST>(accb, FF / 64, buf, lds, f);
    gemm_consume<true, 5, S3_NST>(acca, DM / 64, buf, lds, f);
  }
  const int s = s0 + 16 * wave + (lane & 15);
  const float g = gate[s], bias = bf2f(b2[s]);
#pragma unroll
  for (int ct = 0; ct < 5; ++ct) {
    float r[4];
#pragma unroll
    for (int i = 0; i < 4; ++i) r[i] = g * acca[ct][i] + (1.f - g) * (accb[ct][i] + bias);
    u32x2 v = {pk2(r[0], r[1]), pk2(r[2], r[3])};
    *(u32x2*)(out + (size_t)s * DM + d0 + 16 * ct + 4 * (lane >> 4)) = v;
  }
}

extern "C" void kernel_launch(void* const* d_in, const int* in_sizes, int n_in, void* d_out, int out_size, void* d_ws,
                              size_t ws_size, hipStream_t stream) {
  const u16* x = (const u16*)d_in[0];
  const u16* wq = (const u16*)d_in[1];
  const u16* wk = (const u16*)d_in[2];
  const u16* wv = (const u16*)d_in[3];
  const u16* wo = (const u16*)d_in[4];
  const u16* w1 = (const u16*)d_in[6];
  const u16* b1 = (const u16*)d_in[7];
  const u16* w2 = (const u16*)d_in[8];
  const u16* b2 = (const u16*)d_in[9];
  const u16* gw = (const u16*)d_in[10];
  char* ws = (char*)d_ws;
  (void)hipFuncSetAttribute((const void*)stage1_kernel, hipFuncAttributeMaxDynamicSharedMemorySize, S1_LDS);
  (void)hipFuncSetAttribute((const void*)stage3_kernel, hipFuncAttributeMaxDynamicSharedMemorySize, S3_LDS);
  stage1_kernel<<<S1_QT + S1_KVT + S1_LNT, 512, S1_LDS, stream>>>(x, wq, wk, wv, gw, ws);
  stage2_kernel<<<G1_WGS + AT_WGS, 512, S2_LDS, stream>>>(w1, b1, ws);
  stage3_kernel<<<256, 512, S3_LDS, stream>>>(wo, w2, b2, ws, (u16*)d_out);
}
```

```cpp
#include <hip/hip_runtime.h>
#include <hip/hip_bf16.h>
#include <stdint.h>

typedef short bf16x8 __attribute__((ext_vector_type(8)));
typedef short bf16x4 __attribute__((ext_vector_type(4)));
typedef float f32x16 __attribute__((ext_vector_type(16)));
typedef float f32x4 __attribute__((ext_vector_type(4)));
typedef float f32x2 __attribute__((ext_vector_type(2)));
typedef __bf16 bfx2 __attribute__((ext_vector_type(2)));
typedef unsigned u32x4 __attribute__((ext_vector_type(4)));
typedef unsigned u32x2 __attribute__((ext_vector_type(2)));
typedef unsigned short u16;
typedef __attribute__((address_space(3))) bf16x4* lds_b4p;

#define DI __device__ __forceinline__
#define MFMA32(a, b, c) __builtin_amdgcn_mfma_f32_32x32x16_bf16((a), (b), (c), 0, 0, 0)
#define MFMA32K8(a, b, c) __builtin_amdgcn_mfma_f32_32x32x8bf16_1k((a), (b), (c), 0, 0, 0)
#define MFMA16(a, b, c) __builtin_amdgcn_mfma_f32_16x16x32_bf16((a), (b), (c), 0, 0, 0)

constexpr int SEQ = 2048, DM = 640, NH = 32, DH = 20, FF = 1536;
constexpr int KVCH = 128;
constexpr int KV_KBYTES = KVCH * 48;
constexpr int KV_VSTR = 704;
constexpr int KV_CHBYTES = 12288;
constexpr int NCH = SEQ / KVCH;

constexpr size_t WS_Q = 0;
constexpr size_t WS_H = WS_Q + (size_t)SEQ * DM * 2;
constexpr size_t WS_O = WS_H + (size_t)SEQ * DM * 2;
constexpr size_t WS_U = WS_O + (size_t)SEQ * DM * 2;
constexpr size_t WS_KV = WS_U + (size_t)FF * DM * 2;
constexpr size_t WS_G = WS_KV + (size_t)NCH * KV_CHBYTES;

DI unsigned pk2(float lo, float hi) {
  f32x2 v = {lo, hi};
  bfx2 b = __builtin_convertvector(v, bfx2);
  return __builtin_bit_cast(unsigned, b);
}
DI float bf2f(u16 v) { return __uint_as_float(((unsigned)v) << 16); }
DI u16 f2bf(float f) { return (u16)(pk2(f, 0.f) & 0xffffu); }

DI void glds16(const void* g, unsigned ldsaddr) {
  unsigned keep;
  asm volatile("s_mov_b32 %0, m0\n\ts_mov_b32 m0, %2\n\ts_nop 0\n\tglobal_load_lds_dwordx4 %1, off\n\ts_mov_b32 m0, %0"
               : "=&s"(keep)
               : "v"(g), "s"(ldsaddr)
               : "memory");
}
DI unsigned lds_addr(const void* p) { return (unsigned)(size_t)p; }

#define WAITV(n) asm volatile("s_waitcnt vmcnt(" #n ")" ::: "memory")
#define BARRIER() do { asm volatile("s_waitcnt lgkmcnt(0)" ::: "memory"); __builtin_amdgcn_s_barrier(); asm volatile("" ::: "memory"); } while (0)

DI bf16x8 tr_pair(const char* p0, const char* p1) {
  bf16x4 lo = __builtin_amdgcn_ds_read_tr16_b64_v4i16((lds_b4p)(p0));
  bf16x4 hi = __builtin_amdgcn_ds_read_tr16_b64_v4i16((lds_b4p)(p1));
  return __builtin_shufflevector(lo, hi, 0, 1, 2, 3, 4, 5, 6, 7);
}

constexpr int GSTG = 18432;
template <int NCT> constexpr int gboff() { return NCT == 5 ? 10240 : 8192; }
template <int NCT> constexpr int gstg() { return gboff<NCT>() + 8192; }
DI int colswz(int r) { return (((r >> 1) & 1) | (((r >> 3) & 1) << 1)) << 1; }

struct GemmSeg {
  const u16* src[5];
  size_t astep, bstep;
  int nk, kcur;
};
template <bool BROW>
DI void seg_init(GemmSeg& sg, const u16* A, int lda, const u16* B, int ldb, int nk, int k0, int tid) {
  sg.astep = (size_t)64 * lda;
  sg.bstep = BROW ? (size_t)64 : (size_t)64 * ldb;
  sg.nk = nk;
  sg.kcur = k0;
  A += (size_t)k0 * sg.astep;
  B += (size_t)k0 * sg.bstep;
#pragma unroll
  for (int i = 0; i < 2; ++i) {
    const int c = tid + 256 * i, r = c >> 3, ch = (c & 7) ^ colswz(r);
    sg.src[i] = A + (size_t)r * lda + ch * 8;
    if (BROW) sg.src[2 + i] = B + (size_t)r * ldb + ((c & 7) ^ ((r >> 1) & 7)) * 8;
    else sg.src[2 + i] = B + (size_t)r * ldb + ch * 8;
  }
  const int pos = (tid & 127) >> 1, r = pos ^ (((pos >> 3) & 1) << 2);
  sg.src[4] = A + (size_t)r * lda + 64 + (tid & 1) * 8;
}
template <int NCT>
DI void seg_stage(GemmSeg& sg, unsigned b  , int wave) {
  glds16(sg.src[0], b);
  glds16(sg.src[1], b + 4096);
  glds16(sg.src[2], b + gboff<NCT>());
  glds16(sg.src[3], b + gboff<NCT>() + 4096);
  if (NCT == 5 && wave < 2) glds16(sg.src[4], b + 8192);
  sg.kcur += 1;
  if (sg.kcur == sg.nk) {
    sg.kcur = 0;
    const size_t ab = sg.astep * (sg.nk - 1), bb = sg.bstep * (sg.nk - 1);
    sg.src[0] -= ab; sg.src[1] -= ab; sg.src[4] -= ab;
    sg.src[2] -= bb; sg.src[3] -= bb;
  } else {
    sg.src[0] += sg.astep; sg.src[1] += sg.astep; sg.src[4] += sg.astep;
    sg.src[2] += sg.bstep; sg.src[3] += sg.bstep;
  }
}
struct FragAddr { int aoff, asw, a2off, boffc, boffr, bsw, g; };
DI FragAddr frag_addr(int wave, int lane) {
  FragAddr f;
  const int g = lane >> 4, q = (lane & 15) >> 2, p = lane & 3;
  const int kr = 8 * g + q;
  f.g = g;
  f.aoff = kr * 128 + 8 * p;
  f.asw = colswz(kr);
  f.a2off = 8192 + (kr ^ (((kr >> 3) & 1) << 2)) * 32 + 8 * p;
  const int row = 16 * wave + (lane & 15);
  f.boffr = row * 128;
  f.boffc = kr * 128 + (((2 * wave) ^ f.asw) << 4) + 8 * p;
  f.bsw = (row >> 1) & 7;
  return f;
}
template <bool BROW, int NCT>
DI void gemm_step(f32x4 (&acc)[5], const char* base, const FragAddr& f) {
#pragma unroll
  for (int ks = 0; ks < 2; ++ks) {
    bf16x8 fa[5], fb;
#pragma unroll
    for (int ct = 0; ct < 4; ++ct) {
      const char* a = base + ks * 4096 + f.aoff + (((2 * ct) ^ f.asw) << 4);
      fa[ct] = tr_pair(a, a + 512);
    }
    if (NCT == 5) fa[4] = tr_pair(base + ks * 1024 + f.a2off, base + ks * 1024 + (f.a2off ^ 128));
    const char* bb = base + gboff<NCT>();
    if (BROW) fb = *(const bf16x8*)(bb + f.boffr + (((4 * ks + f.g) ^ f.bsw) << 4));
    else fb = tr_pair(bb + ks * 4096 + f.boffc, bb + ks * 4096 + f.boffc + 512);
#pragma unroll
    for (int ct = 0; ct < NCT; ++ct) acc[ct] = MFMA16(fa[ct], fb, acc[ct]);
  }
}
template <int NCT, int N>
DI void wait_stages(int wave) {
  if (NCT == 5 && wave < 2) asm volatile("s_waitcnt vmcnt(%0)" ::"i"(5 * N) : "memory");
  else asm volatile("s_waitcnt vmcnt(%0)" ::"i"(4 * N) : "memory");
}
template <int NCT, int N>
DI void loader_tail(int lw) {
  wait_stages<NCT, N>(lw);
  __builtin_amdgcn_s_barrier();
  if constexpr (N > 0) loader_tail<NCT, N - 1>(lw);
}
template <int NCT, int NST>
DI void gemm_loader(GemmSeg& s0, int n0, GemmSeg& s1, int n1, char* lds, int lw) {
  const unsigned lbase = lds_addr(lds) + lw * 1024;
#pragma unroll
  for (int i = 0; i < NST - 1; ++i) seg_stage<NCT>(s0, lbase + i * gstg<NCT>(), lw);
  int nb = NST - 1;
  for (int nx = NST - 1; nx < n0; ++nx) {
    wait_stages<NCT, NST - 2>(lw);
    __builtin_amdgcn_s_barrier();
    seg_stage<NCT>(s0, lbase + nb * gstg<NCT>(), lw);
    nb = nb == NST - 1 ? 0 : nb + 1;
  }
  for (int nx = 0; nx < n1; ++nx) {
    wait_stages<NCT, NST - 2>(lw);
    __builtin_amdgcn_s_barrier();
    seg_stage<NCT>(s1, lbase + nb * gstg<NCT>(), lw);
    nb = nb == NST - 1 ? 0 : nb + 1;
  }
  loader_tail<NCT, NST - 2>(lw);
}
template <bool BROW, int NCT, int NST>
DI void gemm_consume(f32x4 (&acc)[5], int nk, int& buf, char* lds, const FragAddr& f) {
  for (int kt = 0; kt < nk; ++kt) {
    BARRIER();
    gemm_step<BROW, NCT>(acc, lds + buf * gstg<NCT>(), f);
    buf = buf == NST - 1 ? 0 : buf + 1;
  }
}

constexpr int S1_NST = 4;
constexpr int S1_QT = 256;
constexpr int S1_KVT = 2 * (SEQ / 32);
constexpr int S1_LDS = S1_NST * GSTG;

__global__ __launch_bounds__(512) void stage1_kernel(const u16* __restrict__ x, const u16* __restrict__ wq,
                                                     const u16* __restrict__ wk, const u16* __restrict__ wv,
                                                     const u16* __restrict__ gw, char* __restrict__ ws) {
  extern __shared__ __attribute__((aligned(16))) char lds[];
  const int tid = threadIdx.x, lane = tid & 63;
  const int wave = __builtin_amdgcn_readfirstlane(tid >> 6);
  int bid = blockIdx.x;
  if (bid < S1_QT) {
    const int xcd = bid & 7, j = bid >> 3;
    const int ts = xcd * 4 + (j >> 3), tn = j & 7;
    const int n0 = tn * 80, s0 = ts * 64;
    if (wave >= 4) {
      GemmSeg sg;
      seg_init<true>(sg, wq + n0, DM, x + (size_t)s0 * DM, DM, DM / 64, (tn + 3 * (j >> 3)) % 10, tid - 256);
      gemm_loader<5, S1_NST>(sg, DM / 64, sg, 0, lds, wave - 4);
      return;
    }
    f32x4 acc[5] = {};
    {
      const FragAddr f = frag_addr(wave, lane);
      int buf = 0;
      gemm_consume<true, 5, S1_NST>(acc, DM / 64, buf, lds, f);
    }
    const float qs = 1.4426950408889634f * 0.22360679774997896f;
    u16* q = (u16*)(ws + WS_Q);
    const int s = s0 + 16 * wave + (lane & 15);
#pragma unroll
    for (int ct = 0; ct < 5; ++ct) {
      u32x2 v = {pk2(acc[ct][0] * qs, acc[ct][1] * qs), pk2(acc[ct][2] * qs, acc[ct][3] * qs)};
      *(u32x2*)(q + (size_t)s * DM + n0 + 16 * ct + 4 * (lane >> 4)) = v;
    }
    return;
  }
  bid -= S1_QT;
  {
    const int isv = bid & 1, s0 = (bid >> 1) * 32;
    char* ximg = lds;
    char* wimg = lds + 40960;
    {
      const u16* w = isv ? wv : wk;
      for (int i = wave; i < 65; i += 8) {
        if (i < 40) {
          const int kt = i >> 2, row = (i & 3) * 8 + (lane >> 3);
          const int c = (lane & 7) ^ ((row >> 1) & 7);
          glds16(x + (size_t)(s0 + row) * DM + kt * 64 + c * 8, lds_addr(ximg) + i * 1024);
        } else {
          glds16((const char*)w + (i - 40) * 1024 + lane * 16, lds_addr(wimg) + (i - 40) * 1024);
        }
      }
    }
    WAITV(0);
    BARRIER();
    const int hh = lane >> 5;
    if (wave < 4) {
      const int i16 = lane & 15, q4 = i16 >> 2, p4 = i16 & 3, g1 = (lane >> 4) & 1;
      const int row = lane & 31;
      f32x16 acc = {};
#pragma unroll
      for (int i = 0; i < 10; ++i) {
        const int k16 = wave * 10 + i;
        const int krow = 16 * k16 + 8 * hh + q4;
        const char* ap = wimg + krow * 40 + (16 * g1 + 4 * p4) * 2;
        bf16x8 fa = tr_pair(ap, ap + 4 * 40);
        const int c = 2 * (k16 & 3) + hh;
        bf16x8 fb = *(const bf16x8*)(ximg + (k16 >> 2) * 4096 + row * 128 + ((c ^ ((row >> 1) & 7)) << 4));
        acc = MFMA32(fa, fb, acc);
      }
      float* red = (float*)(lds + 40960 + 26624);
      (void)red;
      BARRIER();
      float* rb = (float*)lds;
      if (wave > 0) {
#pragma unroll
        for (int i = 0; i < 12; ++i) rb[((wave - 1) * 12 + i) * 64 + lane] = acc[i];
      }
      BARRIER();
      if (wave == 0) {
#pragma unroll
        for (int i = 0; i < 12; ++i) acc[i] += rb[i * 64 + lane] + rb[(12 + i) * 64 + lane] + rb[(24 + i) * 64 + lane];
        const int s = s0 + row;
        char* kvp = ws + WS_KV + (size_t)(s / KVCH) * KV_CHBYTES;
        const int key = s % KVCH;
        const int kb = key >> 4, r = key & 15, vh = (r >> 2) & 1, vj = (r & 3) + 4 * (r >> 3);
        char* vbase = kvp + KV_KBYTES + kb * KV_VSTR + vh * 352 + vj * 2;
#pragma unroll
        for (int g = 0; g < 3; ++g) {
          const int c0 = 8 * g + 4 * hh;
          if (c0 < 20) {
            if (!isv) {
              u32x2 v = {pk2(acc[4 * g], acc[4 * g + 1]), pk2(acc[4 * g + 2], acc[4 * g + 3])};
              *(u32x2*)(kvp + key * 48 + c0 * 2) = v;
            } else {
#pragma unroll
              for (int j = 0; j < 4; ++j) *(u16*)(vbase + (c0 + j) * 16) = f2bf(acc[4 * g + j]);
            }
          }
        }
        if (hh == 1) {
          if (!isv) {
            u32x2 v = {0x00003f80u, 0u};
            *(u32x2*)(kvp + key * 48 + 40) = v;
          } else {
            *(u16*)(vbase + 20 * 16) = 0x3f80;
            *(u16*)(vbase + 21 * 16) = 0;
          }
        }
      }
    } else {
      u16* h = (u16*)(ws + WS_H);
      float* gate = (float*)(ws + WS_G);
      const bool two = lane < 16;
      const u32x4 g0 = *(const u32x4*)(gw + lane * 8);
      u32x4 g1 = {0u, 0u, 0u, 0u};
      if (two) g1 = *(const u32x4*)(gw + 512 + lane * 8);
      u32x4 xa[4], xb[4];
#pragma unroll
      for (int rr = 0; rr < 4; ++rr) {
        const int row = isv * 16 + (wave - 4) * 4 + rr;
        const int sw = (row >> 1) & 7;
        xa[rr] = *(const u32x4*)(ximg + (lane >> 3) * 4096 + row * 128 + (((lane & 7) ^ sw) << 4));
        xb[rr] = u32x4{0u, 0u, 0u, 0u};
        if (two) xb[rr] = *(const u32x4*)(ximg + (8 + (lane >> 3)) * 4096 + row * 128 + (((lane & 7) ^ sw) << 4));
      }
      BARRIER();
      BARRIER();
#pragma unroll
      for (int rr = 0; rr < 4; ++rr) {
        const int s = s0 + isv * 16 + (wave - 4) * 4 + rr;
        float v[16];
        float sum = 0.f, dot = 0.f;
#pragma unroll
        for (int i = 0; i < 4; ++i) {
          v[2 * i] = __uint_as_float(xa[rr][i] << 16);
          v[2 * i + 1] = __uint_as_float(xa[rr][i] & 0xffff0000u);
          v[8 + 2 * i] = __uint_as_float(xb[rr][i] << 16);
          v[8 + 2 * i + 1] = __uint_as_float(xb[rr][i] & 0xffff0000u);
          dot += v[2 * i] * __uint_as_float(g0[i] << 16) + v[2 * i + 1] * __uint_as_float(g0[i] & 0xffff0000u);
          dot += v[8 + 2 * i] * __uint_as_float(g1[i] << 16) + v[8 + 2 * i + 1] * __uint_as_float(g1[i] & 0xffff0000u);
        }
#pragma unroll
        for (int i = 0; i < 16; ++i) sum += v[i];
#pragma unroll
        for (int o = 32; o >= 1; o >>= 1) { sum += __shfl_xor(sum, o); dot += __shfl_xor(dot, o); }
        const float mean = sum * (1.f / DM);
        float var = 0.f;
#pragma unroll
        for (int i = 0; i < 8; ++i) { const float d = v[i] - mean; var += d * d; }
        if (two) {
#pragma unroll
          for (int i = 8; i < 16; ++i) { const float d = v[i] - mean; var += d * d; }
        }
#pragma unroll
        for (int o = 32; o >= 1; o >>= 1) var += __shfl_xor(var, o);
        const float rstd = 1.f / sqrtf(var * (1.f / DM) + 1e-5f);
        u32x4 o0, o1;
#pragma unroll
        for (int i = 0; i < 4; ++i) {
          o0[i] = pk2((v[2 * i] - mean) * rstd, (v[2 * i + 1] - mean) * rstd);
          o1[i] = pk2((v[8 + 2 * i] - mean) * rstd, (v[8 + 2 * i + 1] - mean) * rstd);
        }
        *(u32x4*)(h + (size_t)s * DM + lane * 8) = o0;
        if (two) *(u32x4*)(h + (size_t)s * DM + 512 + lane * 8) = o1;
        if (lane == 0) gate[s] = 1.f / (1.f + __expf(-dot));
      }
    }
  }
}

constexpr int G1_WGS = 240;
constexpr int AT_WGS = SEQ / 8;
constexpr int G1_NST = 3;
constexpr int S2_FLAG = 4 * KV_CHBYTES;
constexpr int S2_LDS = S2_FLAG + 64;

template <bool SAFE>
DI void attn_pass(const char* __restrict__ ws, char* lds, int srow, f32x16& o, float& shift, int wave, int lane) {
  const int hd = lane & 31, hh = lane >> 5;
  const int half = wave >> 3;
  const char* kvg = ws + WS_KV;
  bf16x8 qf0;
  bf16x4 qf1;
  {
    const u16* q = (const u16*)(ws + WS_Q) + (size_t)srow * DM + hd * DH;
    u32x2 a = *(const u32x2*)(q + 8 * hh), b = *(const u32x2*)(q + 8 * hh + 4), c = *(const u32x2*)(q + 16);
    u32x4 t0 = {a[0], a[1], b[0], b[1]};
    u32x2 t1 = {hh == 0 ? c[0] : 0u, hh == 0 ? c[1] : 0u};
    qf0 = __builtin_bit_cast(bf16x8, t0);
    qf1 = __builtin_bit_cast(bf16x4, t1);
  }
  const int koff0 = half * KV_CHBYTES + hd * 48 + 16 * hh, koff1 = half * KV_CHBYTES + hd * 48 + 32 + 8 * hh;
  const int voff = half * KV_CHBYTES + KV_KBYTES + hh * 352 + (hd < 21 ? hd : 21) * 16;
  const int pc0 = wave, pc1 = 16 + wave;
  const char* gsrc0 = kvg + (size_t)((pc0 / 12) * (NCH / 2)) * KV_CHBYTES + (pc0 % 12) * 1024 + lane * 16;
  const int gd1 = (NCH / 2) * KV_CHBYTES + (pc1 - 12) * 1024 - pc0 * 1024;
  const unsigned ldst = lds_addr(lds);
  auto stage = [&](int buf) {
    glds16(gsrc0, ldst + buf * (2 * KV_CHBYTES) + pc0 * 1024);
    if (wave < 8) glds16(gsrc0 + gd1, ldst + buf * (2 * KV_CHBYTES) + pc1 * 1024);
    gsrc0 += KV_CHBYTES;
  };
  float mrun = -3.0e38f;
  stage(0);
  for (int ch = 0; ch < NCH / 2; ++ch) {
    WAITV(0);
    BARRIER();
    if (ch + 1 < NCH / 2) stage((ch + 1) & 1);
    const char* base = lds + (ch & 1) * (2 * KV_CHBYTES);
    bf16x8 k0 = *(const bf16x8*)(base + koff0);
    bf16x4 k1 = *(const bf16x4*)(base + koff1);
    bf16x8 v0 = *(const bf16x8*)(base + voff);
    bf16x8 v1 = *(const bf16x8*)(base + voff + KV_VSTR);
    if (!SAFE && ch == 0) {
      f32x16 z = {};
      f32x16 s = MFMA32(k0, qf0, z);
      s = MFMA32K8(k1, qf1, s);
      float m = s[0];
#pragma unroll
      for (int i = 1; i < 16; ++i) m = fmaxf(m, s[i]);
      m = fmaxf(m, __shfl_xor(m, 32));
      const u16 mb = f2bf(m);
      shift = bf2f(mb);
      if (hh == 1) qf1[0] = (short)(mb ^ 0x8000u);
    }
#pragma unroll SAFE ? 1 : 4
    for (int t = 0; t < KVCH / 32; ++t) {
      f32x16 z = {};
      f32x16 s = MFMA32(k0, qf0, z);
      s = MFMA32K8(k1, qf1, s);
      bf16x8 cv0 = v0, cv1 = v1;
      if (t + 1 < KVCH / 32) {
        k0 = *(const bf16x8*)(base + (t + 1) * 32 * 48 + koff0);
        k1 = *(const bf16x4*)(base + (t + 1) * 32 * 48 + koff1);
        v0 = *(const bf16x8*)(base + voff + (2 * t + 2) * KV_VSTR);
        v1 = *(const bf16x8*)(base + voff + (2 * t + 3) * KV_VSTR);
      }
      if (SAFE) {
        float m = s[0];
#pragma unroll
        for (int i = 1; i < 16; ++i) m = fmaxf(m, s[i]);
        m = fmaxf(m, __shfl_xor(m, 32));
        const float mn = fmaxf(mrun, m);
        const float alpha = __builtin_amdgcn_exp2f(mrun - mn);
        mrun = mn;
#pragma unroll
        for (int i = 0; i < 16; ++i) { o[i] *= alpha; s[i] -= mn; }
      }
      u32x4 p0, p1;
#pragma unroll
      for (int i = 0; i < 4; ++i) {
        p0[i] = pk2(__builtin_amdgcn_exp2f(s[2 * i]), __builtin_amdgcn_exp2f(s[2 * i + 1]));
        p1[i] = pk2(__builtin_amdgcn_exp2f(s[8 + 2 * i]), __builtin_amdgcn_exp2f(s[8 + 2 * i + 1]));
      }
      o = MFMA32(cv0, __builtin_bit_cast(bf16x8, p0), o);
      o = MFMA32(cv1, __builtin_bit_cast(bf16x8, p1), o);
    }
  }
  if (SAFE) shift = mrun;
}

__global__ __launch_bounds__(1024, 6) void stage2_kernel(const u16* __restrict__ w1, const u16* __restrict__ b1,
                                                        char* __restrict__ ws) {
  extern __shared__ __attribute__((aligned(16))) char lds[];
  const int tid = threadIdx.x, lane = tid & 63;
  const int wave = __builtin_amdgcn_readfirstlane(tid >> 6);
  if (blockIdx.x < G1_WGS) {
    if (wave >= 8) return;
    const int bid = blockIdx.x;
    const int xcd = bid & 7, j = bid >> 3;
    const int tf = xcd * 3 + j / 10, td = j % 10;
    const int d0 = td * 64, f0 = tf * 64;
    const u16* h = (const u16*)(ws + WS_H);
    u16* u = (u16*)(ws + WS_U);
    if (wave >= 4) {
      GemmSeg sg;
      seg_init<false>(sg, h + d0, DM, w1 + f0, FF, SEQ / 64, (td * 3 + (j / 10)) & 31, tid - 256);
      gemm_loader<4, G1_NST>(sg, SEQ / 64, sg, 0, lds, wave - 4);
      return;
    }
    f32x4 acc[5] = {};
    {
      const FragAddr f = frag_addr(wave, lane);
      int buf = 0;
      gemm_consume<false, 4, G1_NST>(acc, SEQ / 64, buf, lds, f);
    }
    const int f = f0 + 16 * wave + (lane & 15);
    const float bias = bf2f(b1[f]);
#pragma unroll
    for (int ct = 0; ct < 4; ++ct) {
      float r[4];
#pragma unroll
      for (int i = 0; i < 4; ++i) {
        const float t = acc[ct][i] + bias;
        const float z = 0.7978845608028654f * (t + 0.044715f * t * t * t);
        r[i] = t / (1.f + __expf(-2.f * z));
      }
      u32x2 v = {pk2(r[0], r[1]), pk2(r[2], r[3])};
      *(u32x2*)(u + (size_t)f * DM + d0 + 16 * ct + 4 * (lane >> 4)) = v;
    }
    return;
  }
  const int half = wave >> 3;
  const int s = (blockIdx.x - G1_WGS) * 8 + (wave & 7);
  f32x16 o = {};
  float shift = 0.f;
  volatile unsigned* flag = (volatile unsigned*)(lds + S2_FLAG);
  if (tid == 0) *flag = 0u;
  attn_pass<false>(ws, lds, s, o, shift, wave, lane);
  int lane2 = lane;
  asm volatile("" : "+v"(lane2));
  const int hd = lane2 & 31, hh = lane2 >> 5;
  {
    const float l = __shfl(o[8], hd + 32);
    const bool bad = !(l > 8.67e-19f && l < 1.15e18f);
    if (bad) *flag = 1u;
    __syncthreads();
    if (__builtin_amdgcn_readfirstlane(*flag) != 0u) {
      o = f32x16{};
      attn_pass<true>(ws, lds, s, o, shift, wave, lane2);
    }
  }
  __syncthreads();
  float* mg = (float*)lds + (wave & 7) * 13 * 64;
  if (half == 1) {
#pragma unroll
    for (int i = 0; i < 12; ++i) mg[i * 64 + lane2] = o[i];
    mg[12 * 64 + lane2] = shift;
  }
  __syncthreads();
  if (half == 0) {
    const float sh1 = mg[12 * 64 + lane2];
    const float mm = fmaxf(shift, sh1);
    const float f0 = __builtin_amdgcn_exp2f(shift - mm), f1 = __builtin_amdgcn_exp2f(sh1 - mm);
#pragma unroll
    for (int i = 0; i < 12; ++i) o[i] = o[i] * f0 + mg[i * 64 + lane2] * f1;
    const float l = __shfl(o[8], hd + 32);
    const float inv = 1.f / l;
    u16* op = (u16*)(ws + WS_O) + (size_t)s * DM + hd * DH + 4 * hh;
#pragma unroll
    for (int g = 0; g < 3; ++g) {
      if (g < 2 || hh == 0) {
        u32x2 v = {pk2(o[4 * g] * inv, o[4 * g + 1] * inv), pk2(o[4 * g + 2] * inv, o[4 * g + 3] * inv)};
        *(u32x2*)(op + 8 * g) = v;
      }
    }
  }
}

constexpr int S3_NST = 4;
constexpr int S3_LDS = S3_NST * GSTG;
__global__ __launch_bounds__(512) void stage3_kernel(const u16* __restrict__ wo, const u16* __restrict__ w2,
                                                     const u16* __restrict__ b2, const char* __restrict__ ws,
                                                     u16* __restrict__ out) {
  extern __shared__ __attribute__((aligned(16))) char lds[];
  const int tid = threadIdx.x, lane = tid & 63;
  const int wave = __builtin_amdgcn_readfirstlane(tid >> 6);
  const int bid = blockIdx.x;
  const int xcd = bid & 7, j = bid >> 3;
  const int ts = xcd * 4 + (j >> 3), td = j & 7;
  const int d0 = td * 80, s0 = ts * 64;
  const u16* o = (const u16*)(ws + WS_O);
  const u16* u = (const u16*)(ws + WS_U);
  const float* gate = (const float*)(ws + WS_G);
  if (wave >= 4) {
    GemmSeg sb, sa;
    seg_init<false>(sb, u + d0, DM, w2 + s0, SEQ, FF / 64, td * 3, tid - 256);
    seg_init<true>(sa, wo + d0, DM, o + (size_t)s0 * DM, DM, DM / 64, (td + 3 * (j >> 3)) % 10, tid - 256);
    gemm_loader<5, S3_NST>(sb, FF / 64, sa, DM / 64, lds, wave - 4);
    return;
  }
  const int s = s0 + 16 * wave + (lane & 15);
  const float g = gate[s], bias = bf2f(b2[s]);
  f32x4 acca[5] = {};
  f32x4 accb[5] = {};
  {
    const FragAddr f = frag_addr(wave, lane);
    int buf = 0;
    gemm_consume<false, 5, S3_NST>(accb, FF / 64, buf, lds, f);
    gemm_consume<true, 5, S3_NST>(acca, DM / 64, buf, lds, f);
  }
#pragma unroll
  for (int ct = 0; ct < 5; ++ct) {
    float r[4];
#pragma unroll
    for (int i = 0; i < 4; ++i) r[i] = g * acca[ct][i] + (1.f - g) * (accb[ct][i] + bias);
    u32x2 v = {pk2(r[0], r[1]), pk2(r[2], r[3])};
    *(u32x2*)(out + (size_t)s * DM + d0 + 16 * ct + 4 * (lane >> 4)) = v;
  }
}

extern "C" void kernel_launch(void* const* d_in, const int* in_sizes, int n_in, void* d_out, int out_size, void* d_ws,
                              size_t ws_size, hipStream_t stream) {
  const u16* x = (const u16*)d_in[0];
  const u16* wq = (const u16*)d_in[1];
  const u16* wk = (const u16*)d_in[2];
  const u16* wv = (const u16*)d_in[3];
  const u16* wo = (const u16*)d_in[4];
  const u16* w1 = (const u16*)d_in[6];
  const u16* b1 = (const u16*)d_in[7];
  const u16* w2 = (const u16*)d_in[8];
  const u16* b2 = (const u16*)d_in[9];
  const u16* gw = (const u16*)d_in[10];
  char* ws = (char*)d_ws;
  (void)hipFuncSetAttribute((const void*)stage1_kernel, hipFuncAttributeMaxDynamicSharedMemorySize, S1_LDS);
  (void)hipFuncSetAttribute((const void*)stage3_kernel, hipFuncAttributeMaxDynamicSharedMemorySize, S3_LDS);
  stage1_kernel<<<S1_QT + S1_KVT, 512, S1_LDS, stream>>>(x, wq, wk, wv, gw, ws);
  stage2_kernel<<<G1_WGS + AT_WGS, 1024, S2_LDS, stream>>>(w1, b1, ws);
  stage3_kernel<<<256, 512, S3_LDS, stream>>>(wo, w2, b2, ws, (u16*)d_out);
}
```

```cpp
#include <hip/hip_runtime.h>
#include <hip/hip_bf16.h>
#include <stdint.h>

typedef short bf16x8 __attribute__((ext_vector_type(8)));
typedef short bf16x4 __attribute__((ext_vector_type(4)));
typedef float f32x16 __attribute__((ext_vector_type(16)));
typedef float f32x4 __attribute__((ext_vector_type(4)));
typedef float f32x2 __attribute__((ext_vector_type(2)));
typedef __bf16 bfx2 __attribute__((ext_vector_type(2)));
typedef unsigned u32x4 __attribute__((ext_vector_type(4)));
typedef unsigned u32x2 __attribute__((ext_vector_type(2)));
typedef unsigned short u16;
typedef __attribute__((address_space(3))) bf16x4* lds_b4p;

#define DI __device__ __forceinline__
#define MFMA32(a, b, c) __builtin_amdgcn_mfma_f32_32x32x16_bf16((a), (b), (c), 0, 0, 0)
#define MFMA32K8(a, b, c) __builtin_amdgcn_mfma_f32_32x32x8bf16_1k((a), (b), (c), 0, 0, 0)
#define MFMA16(a, b, c) __builtin_amdgcn_mfma_f32_16x16x32_bf16((a), (b), (c), 0, 0, 0)

constexpr int SEQ = 2048, DM = 640, NH = 32, DH = 20, FF = 1536;
constexpr int KVCH = 128;
constexpr int KV_KBYTES = KVCH * 48;
constexpr int KV_VSTR = 704;
constexpr int KV_CHBYTES = 12288;
constexpr int NCH = SEQ / KVCH;

constexpr size_t WS_Q = 0;
constexpr size_t WS_H = WS_Q + (size_t)SEQ * DM * 2;
constexpr size_t WS_O = WS_H + (size_t)SEQ * DM * 2;
constexpr size_t WS_U = WS_O + (size_t)SEQ * DM * 2;
constexpr size_t WS_KV = WS_U + (size_t)FF * DM * 2;
constexpr size_t WS_G = WS_KV + (size_t)NCH * KV_CHBYTES;
constexpr size_t WS_CNT = WS_G + (size_t)SEQ * 4;
constexpr int CNT_U = 0, CNT_O = 10, CNT_N = 42;

DI unsigned pk2(float lo, float hi) {
  f32x2 v = {lo, hi};
  bfx2 b = __builtin_convertvector(v, bfx2);
  return __builtin_bit_cast(unsigned, b);
}
DI float bf2f(u16 v) { return __uint_as_float(((unsigned)v) << 16); }
DI u16 f2bf(float f) { return (u16)(pk2(f, 0.f) & 0xffffu); }

DI void glds16(const void* g, unsigned ldsaddr) {
  unsigned keep;
  asm volatile("s_mov_b32 %0, m0\n\ts_mov_b32 m0, %2\n\ts_nop 0\n\tglobal_load_lds_dwordx4 %1, off\n\ts_mov_b32 m0, %0"
               : "=&s"(keep)
               : "v"(g), "s"(ldsaddr)
               : "memory");
}
DI unsigned lds_addr(const void* p) { return (unsigned)(size_t)p; }

#define WAITV(n) asm volatile("s_waitcnt vmcnt(" #n ")" ::: "memory")
#define BARRIER() do { asm volatile("s_waitcnt lgkmcnt(0)" ::: "memory"); __builtin_amdgcn_s_barrier(); asm volatile("" ::: "memory"); } while (0)

DI int fresh_lane() {
  int l;
  asm volatile("v_mbcnt_lo_u32_b32 %0, -1, 0\n\tv_mbcnt_hi_u32_b32 %0, -1, %0" : "=v"(l));
  return l;
}
DI float max_halves(float m) {
  auto r = __builtin_amdgcn_permlane32_swap(__float_as_uint(m), __float_as_uint(m), false, false);
  return fmaxf(__uint_as_float(r[0]), __uint_as_float(r[1]));
}
DI float upper_half(float v) {
  auto r = __builtin_amdgcn_permlane32_swap(__float_as_uint(v), __float_as_uint(v), false, false);
  return __uint_as_float(r[1]);
}
DI bf16x8 tr_pair(const char* p0, const char* p1) {
  bf16x4 lo = __builtin_amdgcn_ds_read_tr16_b64_v4i16((lds_b4p)(p0));
  bf16x4 hi = __builtin_amdgcn_ds_read_tr16_b64_v4i16((lds_b4p)(p1));
  return __builtin_shufflevector(lo, hi, 0, 1, 2, 3, 4, 5, 6, 7);
}

constexpr int GSTG = 18432;
template <int NCT> constexpr int gboff() { return NCT == 5 ? 10240 : 8192; }
template <int NCT> constexpr int gstg() { return gboff<NCT>() + 8192; }
DI int colswz(int r) { return (((r >> 1) & 1) | (((r >> 3) & 1) << 1)) << 1; }

struct GemmSeg {
  const u16* src[5];
  size_t astep, bstep;
  int nk, kcur;
};
template <bool BROW>
DI void seg_init(GemmSeg& sg, const u16* A, int lda, const u16* B, int ldb, int nk, int k0, int tid) {
  sg.astep = (size_t)64 * lda;
  sg.bstep = BROW ? (size_t)64 : (size_t)64 * ldb;
  sg.nk = nk;
  sg.kcur = k0;
  A += (size_t)k0 * sg.astep;
  B += (size_t)k0 * sg.bstep;
#pragma unroll
  for (int i = 0; i < 2; ++i) {
    const int c = tid + 256 * i, r = c >> 3, ch = (c & 7) ^ colswz(r);
    sg.src[i] = A + (size_t)r * lda + ch * 8;
    if (BROW) sg.src[2 + i] = B + (size_t)r * ldb + ((c & 7) ^ ((r >> 1) & 7)) * 8;
    else sg.src[2 + i] = B + (size_t)r * ldb + ch * 8;
  }
  const int pos = (tid & 127) >> 1, r = pos ^ (((pos >> 3) & 1) << 2);
  sg.src[4] = A + (size_t)r * lda + 64 + (tid & 1) * 8;
}
template <int NCT>
DI void seg_stage(GemmSeg& sg, unsigned b  , int wave) {
  glds16(sg.src[0], b);
  glds16(sg.src[1], b + 4096);
  glds16(sg.src[2], b + gboff<NCT>());
  glds16(sg.src[3], b + gboff<NCT>() + 4096);
  if (NCT == 5 && wave < 2) glds16(sg.src[4], b + 8192);
  sg.kcur += 1;
  if (sg.kcur == sg.nk) {
    sg.kcur = 0;
    const size_t ab = sg.astep * (sg.nk - 1), bb = sg.bstep * (sg.nk - 1);
    sg.src[0] -= ab; sg.src[1] -= ab; sg.src[4] -= ab;
    sg.src[2] -= bb; sg.src[3] -= bb;
  } else {
    sg.src[0] += sg.astep; sg.src[1] += sg.astep; sg.src[4] += sg.astep;
    sg.src[2] += sg.bstep; sg.src[3] += sg.bstep;
  }
}
struct FragAddr { int aoff, asw, a2off, boffc, boffr, bsw, g; };
DI FragAddr frag_addr(int wave, int lane) {
  FragAddr f;
  const int g = lane >> 4, q = (lane & 15) >> 2, p = lane & 3;
  const int kr = 8 * g + q;
  f.g = g;
  f.aoff = kr * 128 + 8 * p;
  f.asw = colswz(kr);
  f.a2off = 8192 + (kr ^ (((kr >> 3) & 1) << 2)) * 32 + 8 * p;
  const int row = 16 * wave + (lane & 15);
  f.boffr = row * 128;
  f.boffc = kr * 128 + (((2 * wave) ^ f.asw) << 4) + 8 * p;
  f.bsw = (row >> 1) & 7;
  return f;
}
template <bool BROW, int NCT>
DI void gemm_step(f32x4 (&acc)[5], const char* base, const FragAddr& f) {
#pragma unroll
  for (int ks = 0; ks < 2; ++ks) {
    bf16x8 fa[5], fb;
#pragma unroll
    for (int ct = 0; ct < 4; ++ct) {
      const char* a = base + ks * 4096 + f.aoff + (((2 * ct) ^ f.asw) << 4);
      fa[ct] = tr_pair(a, a + 512);
    }
    if (NCT == 5) fa[4] = tr_pair(base + ks * 1024 + f.a2off, base + ks * 1024 + (f.a2off ^ 128));
    const char* bb = base + gboff<NCT>();
    if (BROW) fb = *(const bf16x8*)(bb + f.boffr + (((4 * ks + f.g) ^ f.bsw) << 4));
    else fb = tr_pair(bb + ks * 4096 + f.boffc, bb + ks * 4096 + f.boffc + 512);
#pragma unroll
    for (int ct = 0; ct < NCT; ++ct) acc[ct] = MFMA16(fa[ct], fb, acc[ct]);
  }
}
template <int NCT, int N>
DI void wait_stages(int wave) {
  if (NCT == 5 && wave < 2) asm volatile("s_waitcnt vmcnt(%0)" ::"i"(5 * N) : "memory");
  else asm volatile("s_waitcnt vmcnt(%0)" ::"i"(4 * N) : "memory");
}
template <int NCT, int N>
DI void loader_tail(int lw) {
  wait_stages<NCT, N>(lw);
  __builtin_amdgcn_s_barrier();
  if constexpr (N > 0) loader_tail<NCT, N - 1>(lw);
}
typedef __attribute__((address_space(1))) unsigned gu32;
typedef __attribute__((address_space(1))) unsigned long long gu64;
DI void wait_count(const char* cnt, unsigned target) {
  gu32* p = (gu32*)cnt;
  for (unsigned spins = 0; spins < (1u << 22); ++spins) {
    if ((unsigned)__builtin_amdgcn_readfirstlane(__hip_atomic_load(p, __ATOMIC_RELAXED, __HIP_MEMORY_SCOPE_AGENT)) >= target)
      break;
    __builtin_amdgcn_s_sleep(8);
  }
  __builtin_amdgcn_fence(__ATOMIC_ACQUIRE, "agent");
  asm volatile("s_waitcnt vmcnt(0)" ::: "memory");
}
DI void store8_dev(void* p, u32x2 v) {
  __hip_atomic_store((gu64*)p, ((unsigned long long)v[1] << 32) | v[0], __ATOMIC_RELAXED, __HIP_MEMORY_SCOPE_AGENT);
}
template <int NCT, int NST>
DI void gemm_loader(GemmSeg& s0, int n0, GemmSeg& s1, int n1, char* lds, int lw, const char* cnt1 = nullptr,
                    unsigned tgt1 = 0) {
  const unsigned lbase = lds_addr(lds) + lw * 1024;
#pragma unroll
  for (int i = 0; i < NST - 1; ++i) seg_stage<NCT>(s0, lbase + i * gstg<NCT>(), lw);
  int nb = NST - 1;
  for (int nx = NST - 1; nx < n0; ++nx) {
    wait_stages<NCT, NST - 2>(lw);
    __builtin_amdgcn_s_barrier();
    seg_stage<NCT>(s0, lbase + nb * gstg<NCT>(), lw);
    nb = nb == NST - 1 ? 0 : nb + 1;
  }
  if (cnt1 != nullptr) {
    loader_tail<NCT, NST - 2>(lw);
    wait_count(cnt1, tgt1);
#pragma unroll
    for (int i = 0; i < NST - 2; ++i) {
      seg_stage<NCT>(s1, lbase + nb * gstg<NCT>(), lw);
      nb = nb == NST - 1 ? 0 : nb + 1;
    }
    for (int nx = NST - 2; nx < n1; ++nx) {
      wait_stages<NCT, NST - 3>(lw);
      __builtin_amdgcn_s_barrier();
      seg_stage<NCT>(s1, lbase + nb * gstg<NCT>(), lw);
      nb = nb == NST - 1 ? 0 : nb + 1;
    }
    loader_tail<NCT, NST - 3>(lw);
    return;
  }
  for (int nx = 0; nx < n1; ++nx) {
    wait_stages<NCT, NST - 2>(lw);
    __builtin_amdgcn_s_barrier();
    seg_stage<NCT>(s1, lbase + nb * gstg<NCT>(), lw);
    nb = nb == NST - 1 ? 0 : nb + 1;
  }
  loader_tail<NCT, NST - 2>(lw);
}
template <bool BROW, int NCT, int NST>
DI void gemm_consume(f32x4 (&acc)[5], int nk, int& buf, char* lds, const FragAddr& f) {
  for (int kt = 0; kt < nk; ++kt) {
    BARRIER();
    gemm_step<BROW, NCT>(acc, lds + buf * gstg<NCT>(), f);
    buf = buf == NST - 1 ? 0 : buf + 1;
  }
}

constexpr int S1_NST = 4;
constexpr int S1_QT = 256;
constexpr int S1_KVT = 2 * (SEQ / 32);
constexpr int S1_LDS = S1_NST * GSTG;

__global__ __launch_bounds__(512) void stage1_kernel(const u16* __restrict__ x, const u16* __restrict__ wq,
                                                     const u16* __restrict__ wk, const u16* __restrict__ wv,
                                                     const u16* __restrict__ gw, char* __restrict__ ws) {
  extern __shared__ __attribute__((aligned(16))) char lds[];
  const int tid = threadIdx.x, lane = tid & 63;
  const int wave = __builtin_amdgcn_readfirstlane(tid >> 6);
  int bid = blockIdx.x;
  if (bid == 0 && tid < CNT_N) *(unsigned*)(ws + WS_CNT + tid * 128) = 0u;
  if (bid >= S1_KVT) {
    bid -= S1_KVT;
    const int xcd = bid & 7, j = bid >> 3;
    const int ts = xcd * 4 + (j >> 3), tn = j & 7;
    const int n0 = tn * 80, s0 = ts * 64;
    if (wave >= 4) {
      GemmSeg sg;
      seg_init<true>(sg, wq + n0, DM, x + (size_t)s0 * DM, DM, DM / 64, (tn + 3 * (j >> 3)) % 10, tid - 256);
      gemm_loader<5, S1_NST>(sg, DM / 64, sg, 0, lds, wave - 4);
      return;
    }
    f32x4 acc[5] = {};
    {
      const FragAddr f = frag_addr(wave, lane);
      int buf = 0;
      gemm_consume<true, 5, S1_NST>(acc, DM / 64, buf, lds, f);
    }
    const float qs = 1.4426950408889634f * 0.22360679774997896f;
    u16* q = (u16*)(ws + WS_Q);
    const int s = s0 + 16 * wave + (lane & 15);
#pragma unroll
    for (int ct = 0; ct < 5; ++ct) {
      u32x2 v = {pk2(acc[ct][0] * qs, acc[ct][1] * qs), pk2(acc[ct][2] * qs, acc[ct][3] * qs)};
      *(u32x2*)(q + (size_t)s * DM + n0 + 16 * ct + 4 * (lane >> 4)) = v;
    }
    return;
  }
  {
    const int isv = bid & 1, s0 = (bid >> 1) * 32;
    char* ximg = lds;
    char* wimg = lds + 40960;
    {
      const u16* w = isv ? wv : wk;
      for (int i = wave; i < 65; i += 8) {
        if (i < 40) {
          const int kt = i >> 2, row = (i & 3) * 8 + (lane >> 3);
          const int c = (lane & 7) ^ ((row >> 1) & 7);
          glds16(x + (size_t)(s0 + row) * DM + kt * 64 + c * 8, lds_addr(ximg) + i * 1024);
        } else {
          glds16((const char*)w + (i - 40) * 1024 + lane * 16, lds_addr(wimg) + (i - 40) * 1024);
        }
      }
    }
    WAITV(0);
    BARRIER();
    const int hh = lane >> 5;
    if (wave < 4) {
      const int i16 = lane & 15, q4 = i16 >> 2, p4 = i16 & 3, g1 = (lane >> 4) & 1;
      const int row = lane & 31;
      f32x16 acc = {};
#pragma unroll
      for (int i = 0; i < 10; ++i) {
        const int k16 = wave * 10 + i;
        const int krow = 16 * k16 + 8 * hh + q4;
        const char* ap = wimg + krow * 40 + (16 * g1 + 4 * p4) * 2;
        bf16x8 fa = tr_pair(ap, ap + 4 * 40);
        const int c = 2 * (k16 & 3) + hh;
        bf16x8 fb = *(const bf16x8*)(ximg + (k16 >> 2) * 4096 + row * 128 + ((c ^ ((row >> 1) & 7)) << 4));
        acc = MFMA32(fa, fb, acc);
      }
      float* red = (float*)(lds + 40960 + 26624);
      (void)red;
      BARRIER();
      float* rb = (float*)lds;
      if (wave > 0) {
#pragma unroll
        for (int i = 0; i < 12; ++i) rb[((wave - 1) * 12 + i) * 64 + lane] = acc[i];
      }
      BARRIER();
      if (wave == 0) {
#pragma unroll
        for (int i = 0; i < 12; ++i) acc[i] += rb[i * 64 + lane] + rb[(12 + i) * 64 + lane] + rb[(24 + i) * 64 + lane];
        const int s = s0 + row;
        char* kvp = ws + WS_KV + (size_t)(s / KVCH) * KV_CHBYTES;
        const int key = s % KVCH;
        const int kb = key >> 4, r = key & 15, vh = (r >> 2) & 1, vj = (r & 3) + 4 * (r >> 3);
        char* vbase = kvp + KV_KBYTES + kb * KV_VSTR + vh * 352 + vj * 2;
#pragma unroll
        for (int g = 0; g < 3; ++g) {
          const int c0 = 8 * g + 4 * hh;
          if (c0 < 20) {
            if (!isv) {
              u32x2 v = {pk2(acc[4 * g], acc[4 * g + 1]), pk2(acc[4 * g + 2], acc[4 * g + 3])};
              *(u32x2*)(kvp + key * 48 + c0 * 2) = v;
            } else {
#pragma unroll
              for (int j = 0; j < 4; ++j) *(u16*)(vbase + (c0 + j) * 16) = f2bf(acc[4 * g + j]);
            }
          }
        }
        if (hh == 1) {
          if (!isv) {
            u32x2 v = {0x00003f80u, 0u};
            *(u32x2*)(kvp + key * 48 + 40) = v;
          } else {
            *(u16*)(vbase + 20 * 16) = 0x3f80;
            *(u16*)(vbase + 21 * 16) = 0;
          }
        }
      }
    } else {
      u16* h = (u16*)(ws + WS_H);
      float* gate = (float*)(ws + WS_G);
      const bool two = lane < 16;
      const u32x4 g0 = *(const u32x4*)(gw + lane * 8);
      u32x4 g1 = {0u, 0u, 0u, 0u};
      if (two) g1 = *(const u32x4*)(gw + 512 + lane * 8);
      u32x4 xa[4], xb[4];
#pragma unroll
      for (int rr = 0; rr < 4; ++rr) {
        const int row = isv * 16 + (wave - 4) * 4 + rr;
        const int sw = (row >> 1) & 7;
        xa[rr] = *(const u32x4*)(ximg + (lane >> 3) * 4096 + row * 128 + (((lane & 7) ^ sw) << 4));
        xb[rr] = u32x4{0u, 0u, 0u, 0u};
        if (two) xb[rr] = *(const u32x4*)(ximg + (8 + (lane >> 3)) * 4096 + row * 128 + (((lane & 7) ^ sw) << 4));
      }
      BARRIER();
      BARRIER();
      float sum[4], dot[4], var[4];
#pragma unroll
      for (int rr = 0; rr < 4; ++rr) {
        float sm = 0.f, dt = 0.f;
#pragma unroll
        for (int i = 0; i < 4; ++i) {
          const float a0 = __uint_as_float(xa[rr][i] << 16), a1 = __uint_as_float(xa[rr][i] & 0xffff0000u);
          const float b0 = __uint_as_float(xb[rr][i] << 16), b1 = __uint_as_float(xb[rr][i] & 0xffff0000u);
          sm += (a0 + a1) + (b0 + b1);
          dt += a0 * __uint_as_float(g0[i] << 16) + a1 * __uint_as_float(g0[i] & 0xffff0000u);
          dt += b0 * __uint_as_float(g1[i] << 16) + b1 * __uint_as_float(g1[i] & 0xffff0000u);
        }
        sum[rr] = sm; dot[rr] = dt;
      }
#pragma unroll
      for (int o = 32; o >= 1; o >>= 1) {
#pragma unroll
        for (int rr = 0; rr < 4; ++rr) { sum[rr] += __shfl_xor(sum[rr], o); dot[rr] += __shfl_xor(dot[rr], o); }
      }
#pragma unroll
      for (int rr = 0; rr < 4; ++rr) {
        const float mean = sum[rr] * (1.f / DM);
        sum[rr] = mean;
        float vr = 0.f;
#pragma unroll
        for (int i = 0; i < 4; ++i) {
          const float a0 = __uint_as_float(xa[rr][i] << 16) - mean, a1 = __uint_as_float(xa[rr][i] & 0xffff0000u) - mean;
          const float b0 = __uint_as_float(xb[rr][i] << 16) - mean, b1 = __uint_as_float(xb[rr][i] & 0xffff0000u) - mean;
          vr += a0 * a0 + a1 * a1;
          if (two) vr += b0 * b0 + b1 * b1;
        }
        var[rr] = vr;
      }
#pragma unroll
      for (int o = 32; o >= 1; o >>= 1) {
#pragma unroll
        for (int rr = 0; rr < 4; ++rr) var[rr] += __shfl_xor(var[rr], o);
      }
#pragma unroll
      for (int rr = 0; rr < 4; ++rr) {
        const int s = s0 + isv * 16 + (wave - 4) * 4 + rr;
        const float mean = sum[rr];
        const float rstd = 1.f / sqrtf(var[rr] * (1.f / DM) + 1e-5f);
        u32x4 o0, o1;
#pragma unroll
        for (int i = 0; i < 4; ++i) {
          o0[i] = pk2((__uint_as_float(xa[rr][i] << 16) - mean) * rstd, (__uint_as_float(xa[rr][i] & 0xffff0000u) - mean) * rstd);
          o1[i] = pk2((__uint_as_float(xb[rr][i] << 16) - mean) * rstd, (__uint_as_float(xb[rr][i] & 0xffff0000u) - mean) * rstd);
        }
        *(u32x4*)(h + (size_t)s * DM + lane * 8) = o0;
        if (two) *(u32x4*)(h + (size_t)s * DM + 512 + lane * 8) = o1;
        if (lane == 0) gate[s] = 1.f / (1.f + __expf(-dot[rr]));
      }
    }
  }
}

constexpr int G1_TILES = 240;
constexpr int GM_WGS = 256;
constexpr int AT_WGS = SEQ / 8;
constexpr int G_NST = 4;
constexpr int AT_STG = 2 * KV_CHBYTES;
constexpr int S2_FLAG = 3 * AT_STG;
constexpr int S2_SHIFT = S2_FLAG + 64;
constexpr int S2_LDS = S2_SHIFT + 4096;

template <bool SAFE>
DI void attn_pass(const char* __restrict__ ws, char* lds, int srow, f32x16& o, int wave, int lane) {
  const int hd = lane & 31, hh = lane >> 5;
  const int half = wave >> 3;
  const char* kvg = ws + WS_KV;
  bf16x8 qf0;
  bf16x4 qf1;
  {
    const u16* q = (const u16*)(ws + WS_Q) + (size_t)srow * DM + hd * DH;
    u32x2 a = *(const u32x2*)(q + 8 * hh), b = *(const u32x2*)(q + 8 * hh + 4), c = *(const u32x2*)(q + 16);
    u32x4 t0 = {a[0], a[1], b[0], b[1]};
    u32x2 t1 = {hh == 0 ? c[0] : 0u, hh == 0 ? c[1] : 0u};
    qf0 = __builtin_bit_cast(bf16x8, t0);
    qf1 = __builtin_bit_cast(bf16x4, t1);
  }
  const int koff0 = half * KV_CHBYTES + hd * 48 + 16 * hh, koff1 = half * KV_CHBYTES + hd * 48 + 32 + 8 * hh;
  const int voff = half * KV_CHBYTES + KV_KBYTES + hh * 352 + (hd < 21 ? hd : 21) * 16;
  const int pc0 = wave, pc1 = 16 + wave;
  const char* gsrc0 = kvg + (size_t)((pc0 / 12) * (NCH / 2)) * KV_CHBYTES + (pc0 % 12) * 1024 + lane * 16;
  const int gd1 = (NCH / 2) * KV_CHBYTES + (pc1 - 12) * 1024 - pc0 * 1024;
  const unsigned ldst = lds_addr(lds);
  auto stage = [&](int boff) {
    glds16(gsrc0, ldst + boff + pc0 * 1024);
    if (wave < 8) glds16(gsrc0 + gd1, ldst + boff + pc1 * 1024);
    gsrc0 += KV_CHBYTES;
  };
  float mrun = -3.0e38f;
  stage(0);
  stage(AT_STG);
  if (wave < 8) WAITV(2); else WAITV(1);
  BARRIER();
  int bcur = 0;
  bf16x8 k0 = *(const bf16x8*)(lds + koff0);
  bf16x4 k1 = *(const bf16x4*)(lds + koff1);
  bf16x8 v0 = *(const bf16x8*)(lds + voff);
  bf16x8 v1 = *(const bf16x8*)(lds + voff + KV_VSTR);
  if (!SAFE) {
    f32x16 z = {};
    f32x16 s = MFMA32(k0, qf0, z);
    s = MFMA32K8(k1, qf1, s);
    float m = s[0];
#pragma unroll
    for (int i = 1; i < 16; ++i) m = fmaxf(m, s[i]);
    m = max_halves(m);
    const u16 mb = f2bf(m);
    *(float*)(lds + S2_SHIFT + (wave * 64 + lane) * 4) = bf2f(mb);
    if (hh == 1) qf1[0] = (short)(mb ^ 0x8000u);
  }
  for (int ch = 0; ch < NCH / 2; ++ch) {
    const char* base = lds + bcur;
    const int bnext = bcur == 2 * AT_STG ? 0 : bcur + AT_STG;
#pragma unroll SAFE ? 1 : 4
    for (int t = 0; t < KVCH / 32; ++t) {
      f32x16 z = {};
      f32x16 s = MFMA32(k0, qf0, z);
      s = MFMA32K8(k1, qf1, s);
      bf16x8 cv0 = v0, cv1 = v1;
      if (t + 1 < KVCH / 32) {
        k0 = *(const bf16x8*)(base + (t + 1) * 32 * 48 + koff0);
        k1 = *(const bf16x4*)(base + (t + 1) * 32 * 48 + koff1);
        v0 = *(const bf16x8*)(base + voff + (2 * t + 2) * KV_VSTR);
        v1 = *(const bf16x8*)(base + voff + (2 * t + 3) * KV_VSTR);
      } else if (ch + 1 < NCH / 2) {
        WAITV(0);
        BARRIER();
        if (ch + 2 < NCH / 2) stage(bnext == 2 * AT_STG ? 0 : bnext + AT_STG);
        const char* nb = lds + bnext;
        k0 = *(const bf16x8*)(nb + koff0);
        k1 = *(const bf16x4*)(nb + koff1);
        v0 = *(const bf16x8*)(nb + voff);
        v1 = *(const bf16x8*)(nb + voff + KV_VSTR);
      }
      if (SAFE) {
        float m = s[0];
#pragma unroll
        for (int i = 1; i < 16; ++i) m = fmaxf(m, s[i]);
        m = max_halves(m);
        const float mn = fmaxf(mrun, m);
        const float alpha = __builtin_amdgcn_exp2f(mrun - mn);
        mrun = mn;
#pragma unroll
        for (int i = 0; i < 16; ++i) { o[i] *= alpha; s[i] -= mn; }
      }
      u32x4 p0, p1;
#pragma unroll
      for (int i = 0; i < 4; ++i) {
        p0[i] = pk2(__builtin_amdgcn_exp2f(s[2 * i]), __builtin_amdgcn_exp2f(s[2 * i + 1]));
        p1[i] = pk2(__builtin_amdgcn_exp2f(s[8 + 2 * i]), __builtin_amdgcn_exp2f(s[8 + 2 * i + 1]));
      }
      o = MFMA32(cv0, __builtin_bit_cast(bf16x8, p0), o);
      o = MFMA32(cv1, __builtin_bit_cast(bf16x8, p1), o);
    }
    bcur = bnext;
  }
  if (SAFE) *(float*)(lds + S2_SHIFT + (wave * 64 + lane) * 4) = mrun;
}

__global__ __launch_bounds__(1024, 6) void stage2_kernel(const u16* __restrict__ w1, const u16* __restrict__ b1,
                                                         const u16* __restrict__ wo, const u16* __restrict__ w2,
                                                         const u16* __restrict__ b2, char* __restrict__ ws,
                                                         u16* __restrict__ out) {
  extern __shared__ __attribute__((aligned(16))) char lds[];
  const int tid = threadIdx.x, lane = tid & 63;
  const int wave = __builtin_amdgcn_readfirstlane(tid >> 6);
  if (blockIdx.x < GM_WGS) {
    if (wave >= 8) return;
    const int bid = blockIdx.x;
    const int xcd = bid & 7, j = bid >> 3;
    const char* cnt = ws + WS_CNT;
    u16* u = (u16*)(ws + WS_U);
    const int ts = xcd * 4 + (j >> 3), td = j & 7;
    const int d0 = td * 80, s0 = ts * 64;
    if (wave >= 4) {
      if (j < 30) {
        const int tf = xcd * 3 + j / 10, t1 = j % 10;
        GemmSeg sg;
        seg_init<false>(sg, (const u16*)(ws + WS_H) + t1 * 64, DM, w1 + tf * 64, FF, SEQ / 64, (t1 * 3 + (j / 10)) & 31,
                        tid - 256);
        gemm_loader<4, G_NST>(sg, SEQ / 64, sg, 0, lds, wave - 4);
        __builtin_amdgcn_s_barrier();
      }
      wait_count(cnt + (CNT_U + d0 / 64) * 128, 24);
      wait_count(cnt + (CNT_U + (d0 + 79) / 64) * 128, 24);
      GemmSeg sb, sa;
      seg_init<false>(sb, u + d0, DM, w2 + s0, SEQ, FF / 64, td * 3, tid - 256);
      seg_init<true>(sa, wo + d0, DM, (const u16*)(ws + WS_O) + (size_t)s0 * DM, DM, DM / 64, (td + 3 * (j >> 3)) % 10,
                     tid - 256);
      gemm_loader<5, G_NST>(sb, FF / 64, sa, DM / 64, lds, wave - 4, cnt + (CNT_O + ts) * 128, 8);
      return;
    }
    const FragAddr f = frag_addr(wave, lane);
    if (j < 30) {
      const int tf = xcd * 3 + j / 10, t1 = j % 10;
      const int fcol = tf * 64 + 16 * wave + (lane & 15);
      const float bias = bf2f(b1[fcol]);
      f32x4 acc[5] = {};
      int buf = 0;
      gemm_consume<false, 4, G_NST>(acc, SEQ / 64, buf, lds, f);
#pragma unroll
      for (int ct = 0; ct < 4; ++ct) {
        float r[4];
#pragma unroll
        for (int i = 0; i < 4; ++i) {
          const float t = acc[ct][i] + bias;
          const float z = 0.7978845608028654f * (t + 0.044715f * t * t * t);
          r[i] = t / (1.f + __expf(-2.f * z));
        }
        u32x2 v = {pk2(r[0], r[1]), pk2(r[2], r[3])};
        store8_dev(u + (size_t)fcol * DM + t1 * 64 + 16 * ct + 4 * (lane >> 4), v);
      }
      WAITV(0);
      BARRIER();
      if (tid == 0)
        __hip_atomic_fetch_add((gu32*)(cnt + (CNT_U + t1) * 128), 1u, __ATOMIC_RELAXED, __HIP_MEMORY_SCOPE_AGENT);
    }
    const int s = s0 + 16 * wave + (lane & 15);
    const float g = ((const float*)(ws + WS_G))[s], bias = bf2f(b2[s]);
    f32x4 acca[5] = {};
    f32x4 accb[5] = {};
    {
      int buf = 0;
      gemm_consume<false, 5, G_NST>(accb, FF / 64, buf, lds, f);
      gemm_consume<true, 5, G_NST>(acca, DM / 64, buf, lds, f);
    }
#pragma unroll
    for (int ct = 0; ct < 5; ++ct) {
      float r[4];
#pragma unroll
      for (int i = 0; i < 4; ++i) r[i] = g * acca[ct][i] + (1.f - g) * (accb[ct][i] + bias);
      u32x2 v = {pk2(r[0], r[1]), pk2(r[2], r[3])};
      *(u32x2*)(out + (size_t)s * DM + d0 + 16 * ct + 4 * (lane >> 4)) = v;
    }
    return;
  }
  const int half = wave >> 3;
  const int s = (blockIdx.x - GM_WGS) * 8 + (wave & 7);
  f32x16 o = {};
  volatile unsigned* flag = (volatile unsigned*)(lds + S2_FLAG);
  if (tid == 0) *flag = 0u;
  attn_pass<false>(ws, lds, s, o, wave, lane);

  {
    const float l = upper_half(o[8]);
    const bool bad = !(l > 8.67e-19f && l < 1.15e18f);
    if (bad) *flag = 1u;
    __syncthreads();
    if (__builtin_amdgcn_readfirstlane(*flag) != 0u) {
      o = f32x16{};
      attn_pass<true>(ws, lds, s, o, wave, fresh_lane());
    }
  }
  const int lane2 = fresh_lane();
  const int hd = lane2 & 31, hh = lane2 >> 5;
  __syncthreads();
  float* mg = (float*)lds + (wave & 7) * 13 * 64;
  if (half == 1) {
#pragma unroll
    for (int i = 0; i < 12; ++i) mg[i * 64 + lane2] = o[i];
  }
  __syncthreads();
  if (half == 0) {
    const float shift = *(const float*)(lds + S2_SHIFT + (wave * 64 + lane2) * 4);
    const float sh1 = *(const float*)(lds + S2_SHIFT + ((wave + 8) * 64 + lane2) * 4);
    const float mm = fmaxf(shift, sh1);
    const float f0 = __builtin_amdgcn_exp2f(shift - mm), f1 = __builtin_amdgcn_exp2f(sh1 - mm);
#pragma unroll
    for (int i = 0; i < 12; ++i) o[i] = o[i] * f0 + mg[i * 64 + lane2] * f1;
    const float l = upper_half(o[8]);
    const float inv = 1.f / l;
    u16* op = (u16*)(ws + WS_O) + (size_t)s * DM + hd * DH + 4 * hh;
#pragma unroll
    for (int g = 0; g < 3; ++g) {
      if (g < 2 || hh == 0) {
        u32x2 v = {pk2(o[4 * g] * inv, o[4 * g + 1] * inv), pk2(o[4 * g + 2] * inv, o[4 * g + 3] * inv)};
        store8_dev(op + 8 * g, v);
      }
    }
    WAITV(0);
  }
  __syncthreads();
  if (wave == 0 && lane2 == 0)
    __hip_atomic_fetch_add((gu32*)(ws + WS_CNT + (CNT_O + (s >> 6)) * 128), 1u, __ATOMIC_RELAXED,
                           __HIP_MEMORY_SCOPE_AGENT);
}

extern "C" void kernel_launch(void* const* d_in, const int* in_sizes, int n_in, void* d_out, int out_size, void* d_ws,
                              size_t ws_size, hipStream_t stream) {
  const u16* x = (const u16*)d_in[0];
  const u16* wq = (const u16*)d_in[1];
  const u16* wk = (const u16*)d_in[2];
  const u16* wv = (const u16*)d_in[3];
  const u16* wo = (const u16*)d_in[4];
  const u16* w1 = (const u16*)d_in[6];
  const u16* b1 = (const u16*)d_in[7];
  const u16* w2 = (const u16*)d_in[8];
  const u16* b2 = (const u16*)d_in[9];
  const u16* gw = (const u16*)d_in[10];
  char* ws = (char*)d_ws;
  (void)hipFuncSetAttribute((const void*)stage1_kernel, hipFuncAttributeMaxDynamicSharedMemorySize, S1_LDS);
  (void)hipFuncSetAttribute((const void*)stage2_kernel, hipFuncAttributeMaxDynamicSharedMemorySize, S2_LDS);
  stage1_kernel<<<S1_QT + S1_KVT, 512, S1_LDS, stream>>>(x, wq, wk, wv, gw, ws);
  stage2_kernel<<<GM_WGS + AT_WGS, 1024, S2_LDS, stream>>>(w1, b1, wo, w2, b2, ws, (u16*)d_out);
}
```

```cpp
#include <hip/hip_runtime.h>
#include <hip/hip_bf16.h>
#include <stdint.h>

typedef short bf16x8 __attribute__((ext_vector_type(8)));
typedef short bf16x4 __attribute__((ext_vector_type(4)));
typedef float f32x16 __attribute__((ext_vector_type(16)));
typedef float f32x4 __attribute__((ext_vector_type(4)));
typedef float f32x2 __attribute__((ext_vector_type(2)));
typedef __bf16 bfx2 __attribute__((ext_vector_type(2)));
typedef unsigned u32x4 __attribute__((ext_vector_type(4)));
typedef unsigned u32x2 __attribute__((ext_vector_type(2)));
typedef unsigned short u16;
typedef __attribute__((address_space(3))) bf16x4* lds_b4p;

#define DI __device__ __forceinline__
#define MFMA32(a, b, c) __builtin_amdgcn_mfma_f32_32x32x16_bf16((a), (b), (c), 0, 0, 0)
#define MFMA32K8(a, b, c) __builtin_amdgcn_mfma_f32_32x32x8bf16_1k((a), (b), (c), 0, 0, 0)
#define MFMA16(a, b, c) __builtin_amdgcn_mfma_f32_16x16x32_bf16((a), (b), (c), 0, 0, 0)

constexpr int SEQ = 2048, DM = 640, NH = 32, DH = 20, FF = 1536;
constexpr int KVCH = 128;
constexpr int KV_KBYTES = KVCH * 48;
constexpr int KV_VSTR = 704;
constexpr int KV_CHBYTES = 12288;
constexpr int NCH = SEQ / KVCH;

constexpr size_t WS_Q = 0;
constexpr size_t WS_H = WS_Q + (size_t)SEQ * DM * 2;
constexpr size_t WS_O = WS_H + (size_t)SEQ * DM * 2;
constexpr size_t WS_U = WS_O + (size_t)SEQ * DM * 2;
constexpr size_t WS_KV = WS_U + (size_t)FF * DM * 2;
constexpr size_t WS_G = WS_KV + (size_t)NCH * KV_CHBYTES;
constexpr size_t WS_CNT = WS_G + (size_t)SEQ * 4;
constexpr int CNT_U = 0, CNT_O = 10, CNT_N = 42;

DI unsigned pk2(float lo, float hi) {
  f32x2 v = {lo, hi};
  bfx2 b = __builtin_convertvector(v, bfx2);
  return __builtin_bit_cast(unsigned, b);
}
DI float bf2f(u16 v) { return __uint_as_float(((unsigned)v) << 16); }
DI u16 f2bf(float f) { return (u16)(pk2(f, 0.f) & 0xffffu); }

DI void glds16(const void* g, unsigned ldsaddr) {
  unsigned keep;
  asm volatile("s_mov_b32 %0, m0\n\ts_mov_b32 m0, %2\n\ts_nop 0\n\tglobal_load_lds_dwordx4 %1, off\n\ts_mov_b32 m0, %0"
               : "=&s"(keep)
               : "v"(g), "s"(ldsaddr)
               : "memory");
}
DI void glds16_dev(const void* g, unsigned ldsaddr) {
  unsigned keep;
  asm volatile("s_mov_b32 %0, m0\n\ts_mov_b32 m0, %2\n\ts_nop 0\n\tglobal_load_lds_dwordx4 %1, off sc1\n\ts_mov_b32 m0, %0"
               : "=&s"(keep)
               : "v"(g), "s"(ldsaddr)
               : "memory");
}
DI unsigned lds_addr(const void* p) { return (unsigned)(size_t)p; }

#define WAITV(n) asm volatile("s_waitcnt vmcnt(" #n ")" ::: "memory")
#define BARRIER() do { asm volatile("s_waitcnt lgkmcnt(0)" ::: "memory"); __builtin_amdgcn_s_barrier(); asm volatile("" ::: "memory"); } while (0)

DI int fresh_lane() {
  int l;
  asm volatile("v_mbcnt_lo_u32_b32 %0, -1, 0\n\tv_mbcnt_hi_u32_b32 %0, -1, %0" : "=v"(l));
  return l;
}
DI float max_halves(float m) {
  auto r = __builtin_amdgcn_permlane32_swap(__float_as_uint(m), __float_as_uint(m), false, false);
  return fmaxf(__uint_as_float(r[0]), __uint_as_float(r[1]));
}
DI float upper_half(float v) {
  auto r = __builtin_amdgcn_permlane32_swap(__float_as_uint(v), __float_as_uint(v), false, false);
  return __uint_as_float(r[1]);
}
DI bf16x8 tr_pair(const char* p0, const char* p1) {
  bf16x4 lo = __builtin_amdgcn_ds_read_tr16_b64_v4i16((lds_b4p)(p0));
  bf16x4 hi = __builtin_amdgcn_ds_read_tr16_b64_v4i16((lds_b4p)(p1));
  return __builtin_shufflevector(lo, hi, 0, 1, 2, 3, 4, 5, 6, 7);
}

constexpr int GSTG = 18432;
template <int NCT> constexpr int gboff() { return NCT == 5 ? 10240 : 8192; }
template <int NCT> constexpr int gstg() { return gboff<NCT>() + 8192; }
DI int colswz(int r) { return (((r >> 1) & 1) | (((r >> 3) & 1) << 1)) << 1; }

struct GemmSeg {
  const u16* src[5];
  size_t astep, bstep;
  int nk, kcur;
};
template <bool BROW>
DI void seg_init(GemmSeg& sg, const u16* A, int lda, const u16* B, int ldb, int nk, int k0, int tid) {
  sg.astep = (size_t)64 * lda;
  sg.bstep = BROW ? (size_t)64 : (size_t)64 * ldb;
  sg.nk = nk;
  sg.kcur = k0;
  A += (size_t)k0 * sg.astep;
  B += (size_t)k0 * sg.bstep;
#pragma unroll
  for (int i = 0; i < 2; ++i) {
    const int c = tid + 256 * i, r = c >> 3, ch = (c & 7) ^ colswz(r);
    sg.src[i] = A + (size_t)r * lda + ch * 8;
    if (BROW) sg.src[2 + i] = B + (size_t)r * ldb + ((c & 7) ^ ((r >> 1) & 7)) * 8;
    else sg.src[2 + i] = B + (size_t)r * ldb + ch * 8;
  }
  const int pos = (tid & 127) >> 1, r = pos ^ (((pos >> 3) & 1) << 2);
  sg.src[4] = A + (size_t)r * lda + 64 + (tid & 1) * 8;
}
template <int NCT, int DEV>
DI void seg_stage(GemmSeg& sg, unsigned b  , int wave) {
  if (DEV == 1) { glds16_dev(sg.src[0], b); glds16_dev(sg.src[1], b + 4096); }
  else { glds16(sg.src[0], b); glds16(sg.src[1], b + 4096); }
  if (DEV == 2) { glds16_dev(sg.src[2], b + gboff<NCT>()); glds16_dev(sg.src[3], b + gboff<NCT>() + 4096); }
  else { glds16(sg.src[2], b + gboff<NCT>()); glds16(sg.src[3], b + gboff<NCT>() + 4096); }
  if (NCT == 5 && wave < 2) {
    if (DEV == 1) glds16_dev(sg.src[4], b + 8192);
    else glds16(sg.src[4], b + 8192);
  }
  sg.kcur += 1;
  if (sg.kcur == sg.nk) {
    sg.kcur = 0;
    const size_t ab = sg.astep * (sg.nk - 1), bb = sg.bstep * (sg.nk - 1);
    sg.src[0] -= ab; sg.src[1] -= ab; sg.src[4] -= ab;
    sg.src[2] -= bb; sg.src[3] -= bb;
  } else {
    sg.src[0] += sg.astep; sg.src[1] += sg.astep; sg.src[4] += sg.astep;
    sg.src[2] += sg.bstep; sg.src[3] += sg.bstep;
  }
}
struct FragAddr { int aoff, asw, a2off, boffc, boffr, bsw, g; };
DI FragAddr frag_addr(int wave, int lane) {
  FragAddr f;
  const int g = lane >> 4, q = (lane & 15) >> 2, p = lane & 3;
  const int kr = 8 * g + q;
  f.g = g;
  f.aoff = kr * 128 + 8 * p;
  f.asw = colswz(kr);
  f.a2off = 8192 + (kr ^ (((kr >> 3) & 1) << 2)) * 32 + 8 * p;
  const int row = 16 * wave + (lane & 15);
  f.boffr = row * 128;
  f.boffc = kr * 128 + (((2 * wave) ^ f.asw) << 4) + 8 * p;
  f.bsw = (row >> 1) & 7;
  return f;
}
template <bool BROW, int NCT>
DI void gemm_step(f32x4 (&acc)[5], const char* base, const FragAddr& f) {
#pragma unroll
  for (int ks = 0; ks < 2; ++ks) {
    bf16x8 fa[5], fb;
#pragma unroll
    for (int ct = 0; ct < 4; ++ct) {
      const char* a = base + ks * 4096 + f.aoff + (((2 * ct) ^ f.asw) << 4);
      fa[ct] = tr_pair(a, a + 512);
    }
    if (NCT == 5) fa[4] = tr_pair(base + ks * 1024 + f.a2off, base + ks * 1024 + (f.a2off ^ 128));
    const char* bb = base + gboff<NCT>();
    if (BROW) fb = *(const bf16x8*)(bb + f.boffr + (((4 * ks + f.g) ^ f.bsw) << 4));
    else fb = tr_pair(bb + ks * 4096 + f.boffc, bb + ks * 4096 + f.boffc + 512);
#pragma unroll
    for (int ct = 0; ct < NCT; ++ct) acc[ct] = MFMA16(fa[ct], fb, acc[ct]);
  }
}
template <int NCT, int N>
DI void wait_stages(int wave) {
  if (NCT == 5 && wave < 2) asm volatile("s_waitcnt vmcnt(%0)" ::"i"(5 * N) : "memory");
  else asm volatile("s_waitcnt vmcnt(%0)" ::"i"(4 * N) : "memory");
}
template <int NCT, int N>
DI void loader_tail(int lw) {
  wait_stages<NCT, N>(lw);
  __builtin_amdgcn_s_barrier();
  if constexpr (N > 0) loader_tail<NCT, N - 1>(lw);
}
typedef __attribute__((address_space(1))) unsigned gu32;
typedef __attribute__((address_space(1))) unsigned long long gu64;
DI void wait_count(const char* cnt, unsigned target) {
  gu32* p = (gu32*)cnt;
  for (unsigned spins = 0; spins < (1u << 22); ++spins) {
    if ((unsigned)__builtin_amdgcn_readfirstlane(__hip_atomic_load(p, __ATOMIC_RELAXED, __HIP_MEMORY_SCOPE_AGENT)) >= target)
      break;
    __builtin_amdgcn_s_sleep(8);
  }
  __builtin_amdgcn_fence(__ATOMIC_ACQUIRE, "wavefront");
}
DI void store8_dev(void* p, u32x2 v) {
  __hip_atomic_store((gu64*)p, ((unsigned long long)v[1] << 32) | v[0], __ATOMIC_RELAXED, __HIP_MEMORY_SCOPE_AGENT);
}
template <int NCT, int NST, int DEV0, int DEV1>
DI void gemm_loader(GemmSeg& s0, int n0, GemmSeg& s1, int n1, char* lds, int lw, const char* cnt1 = nullptr,
                    unsigned tgt1 = 0) {
  const unsigned lbase = lds_addr(lds) + lw * 1024;
#pragma unroll
  for (int i = 0; i < NST - 1; ++i) seg_stage<NCT, DEV0>(s0, lbase + i * gstg<NCT>(), lw);
  int nb = NST - 1;
  for (int nx = NST - 1; nx < n0; ++nx) {
    wait_stages<NCT, NST - 2>(lw);
    __builtin_amdgcn_s_barrier();
    seg_stage<NCT, DEV0>(s0, lbase + nb * gstg<NCT>(), lw);
    nb = nb == NST - 1 ? 0 : nb + 1;
  }
  if (cnt1 != nullptr) {
    loader_tail<NCT, NST - 2>(lw);
    wait_count(cnt1, tgt1);
#pragma unroll
    for (int i = 0; i < NST - 2; ++i) {
      seg_stage<NCT, DEV1>(s1, lbase + nb * gstg<NCT>(), lw);
      nb = nb == NST - 1 ? 0 : nb + 1;
    }
    for (int nx = NST - 2; nx < n1; ++nx) {
      wait_stages<NCT, NST - 3>(lw);
      __builtin_amdgcn_s_barrier();
      seg_stage<NCT, DEV1>(s1, lbase + nb * gstg<NCT>(), lw);
      nb = nb == NST - 1 ? 0 : nb + 1;
    }
    loader_tail<NCT, NST - 3>(lw);
    return;
  }
  for (int nx = 0; nx < n1; ++nx) {
    wait_stages<NCT, NST - 2>(lw);
    __builtin_amdgcn_s_barrier();
    seg_stage<NCT, DEV1>(s1, lbase + nb * gstg<NCT>(), lw);
    nb = nb == NST - 1 ? 0 : nb + 1;
  }
  loader_tail<NCT, NST - 2>(lw);
}
template <bool BROW, int NCT, int NST>
DI void gemm_consume(f32x4 (&acc)[5], int nk, int& buf, char* lds, const FragAddr& f) {
  for (int kt = 0; kt < nk; ++kt) {
    BARRIER();
    gemm_step<BROW, NCT>(acc, lds + buf * gstg<NCT>(), f);
    buf = buf == NST - 1 ? 0 : buf + 1;
  }
}

constexpr int S1_NST = 4;
constexpr int S1_QT = 256;
constexpr int S1_KVT = 2 * (SEQ / 32);
constexpr int S1_LDS = S1_NST * GSTG;

__global__ __launch_bounds__(512) void stage1_kernel(const u16* __restrict__ x, const u16* __restrict__ wq,
                                                     const u16* __restrict__ wk, const u16* __restrict__ wv,
                                                     const u16* __restrict__ gw, char* __restrict__ ws) {
  extern __shared__ __attribute__((aligned(16))) char lds[];
  const int tid = threadIdx.x, lane = tid & 63;
  const int wave = __builtin_amdgcn_readfirstlane(tid >> 6);
  int bid = blockIdx.x;
  if (bid == 0 && tid < CNT_N) *(unsigned*)(ws + WS_CNT + tid * 128) = 0u;
  if (bid >= S1_KVT) {
    bid -= S1_KVT;
    const int xcd = bid & 7, j = bid >> 3;
    const int ts = xcd * 4 + (j >> 3), tn = j & 7;
    const int n0 = tn * 80, s0 = ts * 64;
    if (wave >= 4) {
      GemmSeg sg;
      seg_init<true>(sg, wq + n0, DM, x + (size_t)s0 * DM, DM, DM / 64, (tn + 3 * (j >> 3)) % 10, tid - 256);
      gemm_loader<5, S1_NST, 0, 0>(sg, DM / 64, sg, 0, lds, wave - 4);
      return;
    }
    f32x4 acc[5] = {};
    {
      const FragAddr f = frag_addr(wave, lane);
      int buf = 0;
      gemm_consume<true, 5, S1_NST>(acc, DM / 64, buf, lds, f);
    }
    const float qs = 1.4426950408889634f * 0.22360679774997896f;
    u16* q = (u16*)(ws + WS_Q);
    const int s = s0 + 16 * wave + (lane & 15);
#pragma unroll
    for (int ct = 0; ct < 5; ++ct) {
      u32x2 v = {pk2(acc[ct][0] * qs, acc[ct][1] * qs), pk2(acc[ct][2] * qs, acc[ct][3] * qs)};
      *(u32x2*)(q + (size_t)s * DM + n0 + 16 * ct + 4 * (lane >> 4)) = v;
    }
    return;
  }
  {
    const int isv = bid & 1, s0 = (bid >> 1) * 32;
    char* ximg = lds;
    char* wimg = lds + 40960;
    {
      const u16* w = isv ? wv : wk;
      for (int i = wave; i < 65; i += 8) {
        if (i < 40) {
          const int kt = i >> 2, row = (i & 3) * 8 + (lane >> 3);
          const int c = (lane & 7) ^ ((row >> 1) & 7);
          glds16(x + (size_t)(s0 + row) * DM + kt * 64 + c * 8, lds_addr(ximg) + i * 1024);
        } else {
          glds16((const char*)w + (i - 40) * 1024 + lane * 16, lds_addr(wimg) + (i - 40) * 1024);
        }
      }
    }
    WAITV(0);
    BARRIER();
    const int hh = lane >> 5;
    if (wave < 4) {
      const int i16 = lane & 15, q4 = i16 >> 2, p4 = i16 & 3, g1 = (lane >> 4) & 1;
      const int row = lane & 31;
      f32x16 acc = {};
#pragma unroll
      for (int i = 0; i < 10; ++i) {
        const int k16 = wave * 10 + i;
        const int krow = 16 * k16 + 8 * hh + q4;
        const char* ap = wimg + krow * 40 + (16 * g1 + 4 * p4) * 2;
        bf16x8 fa = tr_pair(ap, ap + 4 * 40);
        const int c = 2 * (k16 & 3) + hh;
        bf16x8 fb = *(const bf16x8*)(ximg + (k16 >> 2) * 4096 + row * 128 + ((c ^ ((row >> 1) & 7)) << 4));
        acc = MFMA32(fa, fb, acc);
      }
      float* red = (float*)(lds + 40960 + 26624);
      (void)red;
      BARRIER();
      float* rb = (float*)lds;
      if (wave > 0) {
#pragma unroll
        for (int i = 0; i < 12; ++i) rb[((wave - 1) * 12 + i) * 64 + lane] = acc[i];
      }
      BARRIER();
      if (wave == 0) {
#pragma unroll
        for (int i = 0; i < 12; ++i) acc[i] += rb[i * 64 + lane] + rb[(12 + i) * 64 + lane] + rb[(24 + i) * 64 + lane];
        const int s = s0 + row;
        char* kvp = ws + WS_KV + (size_t)(s / KVCH) * KV_CHBYTES;
        const int key = s % KVCH;
        const int kb = key >> 4, r = key & 15, vh = (r >> 2) & 1, vj = (r & 3) + 4 * (r >> 3);
        char* vbase = kvp + KV_KBYTES + kb * KV_VSTR + vh * 352 + vj * 2;
#pragma unroll
        for (int g = 0; g < 3; ++g) {
          const int c0 = 8 * g + 4 * hh;
          if (c0 < 20) {
            if (!isv) {
              u32x2 v = {pk2(acc[4 * g], acc[4 * g + 1]), pk2(acc[4 * g + 2], acc[4 * g + 3])};
              *(u32x2*)(kvp + key * 48 + c0 * 2) = v;
            } else {
#pragma unroll
              for (int j = 0; j < 4; ++j) *(u16*)(vbase + (c0 + j) * 16) = f2bf(acc[4 * g + j]);
            }
          }
        }
        if (hh == 1) {
          if (!isv) {
            u32x2 v = {0x00003f80u, 0u};
            *(u32x2*)(kvp + key * 48 + 40) = v;
          } else {
            *(u16*)(vbase + 20 * 16) = 0x3f80;
            *(u16*)(vbase + 21 * 16) = 0;
          }
        }
      }
    } else {
      u16* h = (u16*)(ws + WS_H);
      float* gate = (float*)(ws + WS_G);
      const bool two = lane < 16;
      const u32x4 g0 = *(const u32x4*)(gw + lane * 8);
      u32x4 g1 = {0u, 0u, 0u, 0u};
      if (two) g1 = *(const u32x4*)(gw + 512 + lane * 8);
      u32x4 xa[4], xb[4];
#pragma unroll
      for (int rr = 0; rr < 4; ++rr) {
        const int row = isv * 16 + (wave - 4) * 4 + rr;
        const int sw = (row >> 1) & 7;
        xa[rr] = *(const u32x4*)(ximg + (lane >> 3) * 4096 + row * 128 + (((lane & 7) ^ sw) << 4));
        xb[rr] = u32x4{0u, 0u, 0u, 0u};
        if (two) xb[rr] = *(const u32x4*)(ximg + (8 + (lane >> 3)) * 4096 + row * 128 + (((lane & 7) ^ sw) << 4));
      }
      BARRIER();
      BARRIER();
      float sum[4], dot[4], var[4];
#pragma unroll
      for (int rr = 0; rr < 4; ++rr) {
        float sm = 0.f, dt = 0.f;
#pragma unroll
        for (int i = 0; i < 4; ++i) {
          const float a0 = __uint_as_float(xa[rr][i] << 16), a1 = __uint_as_float(xa[rr][i] & 0xffff0000u);
          const float b0 = __uint_as_float(xb[rr][i] << 16), b1 = __uint_as_float(xb[rr][i] & 0xffff0000u);
          sm += (a0 + a1) + (b0 + b1);
          dt += a0 * __uint_as_float(g0[i] << 16) + a1 * __uint_as_float(g0[i] & 0xffff0000u);
          dt += b0 * __uint_as_float(g1[i] << 16) + b1 * __uint_as_float(g1[i] & 0xffff0000u);
        }
        sum[rr] = sm; dot[rr] = dt;
      }
#pragma unroll
      for (int o = 32; o >= 1; o >>= 1) {
#pragma unroll
        for (int rr = 0; rr < 4; ++rr) { sum[rr] += __shfl_xor(sum[rr], o); dot[rr] += __shfl_xor(dot[rr], o); }
      }
#pragma unroll
      for (int rr = 0; rr < 4; ++rr) {
        const float mean = sum[rr] * (1.f / DM);
        sum[rr] = mean;
        float vr = 0.f;
#pragma unroll
        for (int i = 0; i < 4; ++i) {
          const float a0 = __uint_as_float(xa[rr][i] << 16) - mean, a1 = __uint_as_float(xa[rr][i] & 0xffff0000u) - mean;
          const float b0 = __uint_as_float(xb[rr][i] << 16) - mean, b1 = __uint_as_float(xb[rr][i] & 0xffff0000u) - mean;
          vr += a0 * a0 + a1 * a1;
          if (two) vr += b0 * b0 + b1 * b1;
        }
        var[rr] = vr;
      }
#pragma unroll
      for (int o = 32; o >= 1; o >>= 1) {
#pragma unroll
        for (int rr = 0; rr < 4; ++rr) var[rr] += __shfl_xor(var[rr], o);
      }
#pragma unroll
      for (int rr = 0; rr < 4; ++rr) {
        const int s = s0 + isv * 16 + (wave - 4) * 4 + rr;
        const float mean = sum[rr];
        const float rstd = 1.f / sqrtf(var[rr] * (1.f / DM) + 1e-5f);
        u32x4 o0, o1;
#pragma unroll
        for (int i = 0; i < 4; ++i) {
          o0[i] = pk2((__uint_as_float(xa[rr][i] << 16) - mean) * rstd, (__uint_as_float(xa[rr][i] & 0xffff0000u) - mean) * rstd);
          o1[i] = pk2((__uint_as_float(xb[rr][i] << 16) - mean) * rstd, (__uint_as_float(xb[rr][i] & 0xffff0000u) - mean) * rstd);
        }
        *(u32x4*)(h + (size_t)s * DM + lane * 8) = o0;
        if (two) *(u32x4*)(h + (size_t)s * DM + 512 + lane * 8) = o1;
        if (lane == 0) gate[s] = 1.f / (1.f + __expf(-dot[rr]));
      }
    }
  }
}

constexpr int G1_TILES = 240;
constexpr int GM_WGS = 256;
constexpr int AT_WGS = SEQ / 8;
constexpr int G_NST = 4;
constexpr int AT_STG = 2 * KV_CHBYTES;
constexpr int S2_FLAG = 3 * AT_STG;
constexpr int S2_SHIFT = S2_FLAG + 64;
constexpr int S2_LDS = S2_SHIFT + 4096;

template <bool SAFE>
DI void attn_pass(const char* __restrict__ ws, char* lds, int srow, f32x16& o, int wave, int lane) {
  const int hd = lane & 31, hh = lane >> 5;
  const int half = wave >> 3;
  const char* kvg = ws + WS_KV;
  bf16x8 qf0;
  bf16x4 qf1;
  {
    const u16* q = (const u16*)(ws + WS_Q) + (size_t)srow * DM + hd * DH;
    u32x2 a = *(const u32x2*)(q + 8 * hh), b = *(const u32x2*)(q + 8 * hh + 4), c = *(const u32x2*)(q + 16);
    u32x4 t0 = {a[0], a[1], b[0], b[1]};
    u32x2 t1 = {hh == 0 ? c[0] : 0u, hh == 0 ? c[1] : 0u};
    qf0 = __builtin_bit_cast(bf16x8, t0);
    qf1 = __builtin_bit_cast(bf16x4, t1);
  }
  const int koff0 = half * KV_CHBYTES + hd * 48 + 16 * hh, koff1 = half * KV_CHBYTES + hd * 48 + 32 + 8 * hh;
  const int voff = half * KV_CHBYTES + KV_KBYTES + hh * 352 + (hd < 21 ? hd : 21) * 16;
  const int pc0 = wave, pc1 = 16 + wave;
  const char* gsrc0 = kvg + (size_t)((pc0 / 12) * (NCH / 2)) * KV_CHBYTES + (pc0 % 12) * 1024 + lane * 16;
  const int gd1 = (NCH / 2) * KV_CHBYTES + (pc1 - 12) * 1024 - pc0 * 1024;
  const unsigned ldst = lds_addr(lds);
  auto stage = [&](int boff) {
    glds16(gsrc0, ldst + boff + pc0 * 1024);
    if (wave < 8) glds16(gsrc0 + gd1, ldst + boff + pc1 * 1024);
    gsrc0 += KV_CHBYTES;
  };
  float mrun = -3.0e38f;
  stage(0);
  stage(AT_STG);
  if (wave < 8) WAITV(2); else WAITV(1);
  BARRIER();
  int bcur = 0;
  bf16x8 k0 = *(const bf16x8*)(lds + koff0);
  bf16x4 k1 = *(const bf16x4*)(lds + koff1);
  bf16x8 v0 = *(const bf16x8*)(lds + voff);
  bf16x8 v1 = *(const bf16x8*)(lds + voff + KV_VSTR);
  if (!SAFE) {
    f32x16 z = {};
    f32x16 s = MFMA32(k0, qf0, z);
    s = MFMA32K8(k1, qf1, s);
    float m = s[0];
#pragma unroll
    for (int i = 1; i < 16; ++i) m = fmaxf(m, s[i]);
    m = max_halves(m);
    const u16 mb = f2bf(m);
    *(float*)(lds + S2_SHIFT + (wave * 64 + lane) * 4) = bf2f(mb);
    if (hh == 1) qf1[0] = (short)(mb ^ 0x8000u);
  }
  for (int ch = 0; ch < NCH / 2; ++ch) {
    const char* base = lds + bcur;
    const int bnext = bcur == 2 * AT_STG ? 0 : bcur + AT_STG;
#pragma unroll SAFE ? 1 : 4
    for (int t = 0; t < KVCH / 32; ++t) {
      f32x16 z = {};
      f32x16 s = MFMA32(k0, qf0, z);
      s = MFMA32K8(k1, qf1, s);
      bf16x8 cv0 = v0, cv1 = v1;
      if (t + 1 < KVCH / 32) {
        k0 = *(const bf16x8*)(base + (t + 1) * 32 * 48 + koff0);
        k1 = *(const bf16x4*)(base + (t + 1) * 32 * 48 + koff1);
        v0 = *(const bf16x8*)(base + voff + (2 * t + 2) * KV_VSTR);
        v1 = *(const bf16x8*)(base + voff + (2 * t + 3) * KV_VSTR);
      } else if (ch + 1 < NCH / 2) {
        WAITV(0);
        BARRIER();
        if (ch + 2 < NCH / 2) stage(bnext == 2 * AT_STG ? 0 : bnext + AT_STG);
        const char* nb = lds + bnext;
        k0 = *(const bf16x8*)(nb + koff0);
        k1 = *(const bf16x4*)(nb + koff1);
        v0 = *(const bf16x8*)(nb + voff);
        v1 = *(const bf16x8*)(nb + voff + KV_VSTR);
      }
      if (SAFE) {
        float m = s[0];
#pragma unroll
        for (int i = 1; i < 16; ++i) m = fmaxf(m, s[i]);
        m = max_halves(m);
        const float mn = fmaxf(mrun, m);
        const float alpha = __builtin_amdgcn_exp2f(mrun - mn);
        mrun = mn;
#pragma unroll
        for (int i = 0; i < 16; ++i) { o[i] *= alpha; s[i] -= mn; }
      }
      u32x4 p0, p1;
#pragma unroll
      for (int i = 0; i < 4; ++i) {
        p0[i] = pk2(__builtin_amdgcn_exp2f(s[2 * i]), __builtin_amdgcn_exp2f(s[2 * i + 1]));
        p1[i] = pk2(__builtin_amdgcn_exp2f(s[8 + 2 * i]), __builtin_amdgcn_exp2f(s[8 + 2 * i + 1]));
      }
      o = MFMA32(cv0, __builtin_bit_cast(bf16x8, p0), o);
      o = MFMA32(cv1, __builtin_bit_cast(bf16x8, p1), o);
    }
    bcur = bnext;
  }
  if (SAFE) *(float*)(lds + S2_SHIFT + (wave * 64 + lane) * 4) = mrun;
}

__global__ __launch_bounds__(1024, 6) void stage2_kernel(const u16* __restrict__ w1, const u16* __restrict__ b1,
                                                         const u16* __restrict__ wo, const u16* __restrict__ w2,
                                                         const u16* __restrict__ b2, char* __restrict__ ws,
                                                         u16* __restrict__ out) {
  extern __shared__ __attribute__((aligned(16))) char lds[];
  const int tid = threadIdx.x, lane = tid & 63;
  const int wave = __builtin_amdgcn_readfirstlane(tid >> 6);
  if (blockIdx.x < GM_WGS) {
    if (wave >= 8) return;
    const int bid = blockIdx.x;
    const int xcd = bid & 7, j = bid >> 3;
    const char* cnt = ws + WS_CNT;
    u16* u = (u16*)(ws + WS_U);
    const int ts = xcd * 4 + (j >> 3), td = j & 7;
    const int d0 = td * 80, s0 = ts * 64;
    if (wave >= 4) {
      if (j < 30) {
        const int tf = xcd * 3 + j / 10, t1 = j % 10;
        GemmSeg sg;
        seg_init<false>(sg, (const u16*)(ws + WS_H) + t1 * 64, DM, w1 + tf * 64, FF, SEQ / 64, (t1 * 3 + (j / 10)) & 31,
                        tid - 256);
        gemm_loader<4, G_NST, 0, 0>(sg, SEQ / 64, sg, 0, lds, wave - 4);
        __builtin_amdgcn_s_barrier();
      }
      wait_count(cnt + (CNT_U + d0 / 64) * 128, 24);
      wait_count(cnt + (CNT_U + (d0 + 79) / 64) * 128, 24);
      GemmSeg sb, sa;
      seg_init<false>(sb, u + d0, DM, w2 + s0, SEQ, FF / 64, td * 3, tid - 256);
      seg_init<true>(sa, wo + d0, DM, (const u16*)(ws + WS_O) + (size_t)s0 * DM, DM, DM / 64, (td + 3 * (j >> 3)) % 10,
                     tid - 256);
      gemm_loader<5, G_NST, 1, 2>(sb, FF / 64, sa, DM / 64, lds, wave - 4, cnt + (CNT_O + ts) * 128, 8);
      return;
    }
    const FragAddr f = frag_addr(wave, lane);
    if (j < 30) {
      const int tf = xcd * 3 + j / 10, t1 = j % 10;
      const int fcol = tf * 64 + 16 * wave + (lane & 15);
      const float bias = bf2f(b1[fcol]);
      f32x4 acc[5] = {};
      int buf = 0;
      gemm_consume<false, 4, G_NST>(acc, SEQ / 64, buf, lds, f);
#pragma unroll
      for (int ct = 0; ct < 4; ++ct) {
        float r[4];
#pragma unroll
        for (int i = 0; i < 4; ++i) {
          const float t = acc[ct][i] + bias;
          const float z = 0.7978845608028654f * (t + 0.044715f * t * t * t);
          r[i] = t / (1.f + __expf(-2.f * z));
        }
        u32x2 v = {pk2(r[0], r[1]), pk2(r[2], r[3])};
        store8_dev(u + (size_t)fcol * DM + t1 * 64 + 16 * ct + 4 * (lane >> 4), v);
      }
      WAITV(0);
      BARRIER();
      if (tid == 0)
        __hip_atomic_fetch_add((gu32*)(cnt + (CNT_U + t1) * 128), 1u, __ATOMIC_RELAXED, __HIP_MEMORY_SCOPE_AGENT);
    }
    const int s = s0 + 16 * wave + (lane & 15);
    const float g = ((const float*)(ws + WS_G))[s], bias = bf2f(b2[s]);
    f32x4 acca[5] = {};
    f32x4 accb[5] = {};
    {
      int buf = 0;
      gemm_consume<false, 5, G_NST>(accb, FF / 64, buf, lds, f);
      gemm_consume<true, 5, G_NST>(acca, DM / 64, buf, lds, f);
    }
#pragma unroll
    for (int ct = 0; ct < 5; ++ct) {
      float r[4];
#pragma unroll
      for (int i = 0; i < 4; ++i) r[i] = g * acca[ct][i] + (1.f - g) * (accb[ct][i] + bias);
      u32x2 v = {pk2(r[0], r[1]), pk2(r[2], r[3])};
      *(u32x2*)(out + (size_t)s * DM + d0 + 16 * ct + 4 * (lane >> 4)) = v;
    }
    return;
  }
  const int half = wave >> 3;
  const int s = (blockIdx.x - GM_WGS) * 8 + (wave & 7);
  f32x16 o = {};
  volatile unsigned* flag = (volatile unsigned*)(lds + S2_FLAG);
  if (tid == 0) *flag = 0u;
  attn_pass<false>(ws, lds, s, o, wave, lane);

  {
    const float l = upper_half(o[8]);
    const bool bad = !(l > 8.67e-19f && l < 1.15e18f);
    if (bad) *flag = 1u;
    __syncthreads();
    if (__builtin_amdgcn_readfirstlane(*flag) != 0u) {
      o = f32x16{};
      attn_pass<true>(ws, lds, s, o, wave, fresh_lane());
    }
  }
  const int lane2 = fresh_lane();
  const int hd = lane2 & 31, hh = lane2 >> 5;
  __syncthreads();
  float* mg = (float*)lds + (wave & 7) * 13 * 64;
  if (half == 1) {
#pragma unroll
    for (int i = 0; i < 12; ++i) mg[i * 64 + lane2] = o[i];
  }
  __syncthreads();
  if (half == 0) {
    const float shift = *(const float*)(lds + S2_SHIFT + (wave * 64 + lane2) * 4);
    const float sh1 = *(const float*)(lds + S2_SHIFT + ((wave + 8) * 64 + lane2) * 4);
    const float mm = fmaxf(shift, sh1);
    const float f0 = __builtin_amdgcn_exp2f(shift - mm), f1 = __builtin_amdgcn_exp2f(sh1 - mm);
#pragma unroll
    for (int i = 0; i < 12; ++i) o[i] = o[i] * f0 + mg[i * 64 + lane2] * f1;
    const float l = upper_half(o[8]);
    const float inv = 1.f / l;
    u16* op = (u16*)(ws + WS_O) + (size_t)s * DM + hd * DH + 4 * hh;
#pragma unroll
    for (int g = 0; g < 3; ++g) {
      if (g < 2 || hh == 0) {
        u32x2 v = {pk2(o[4 * g] * inv, o[4 * g + 1] * inv), pk2(o[4 * g + 2] * inv, o[4 * g + 3] * inv)};
        store8_dev(op + 8 * g, v);
      }
    }
    WAITV(0);
  }
  __syncthreads();
  if (wave == 0 && lane2 == 0)
    __hip_atomic_fetch_add((gu32*)(ws + WS_CNT + (CNT_O + (s >> 6)) * 128), 1u, __ATOMIC_RELAXED,
                           __HIP_MEMORY_SCOPE_AGENT);
}

extern "C" void kernel_launch(void* const* d_in, const int* in_sizes, int n_in, void* d_out, int out_size, void* d_ws,
                              size_t ws_size, hipStream_t stream) {
  const u16* x = (const u16*)d_in[0];
  const u16* wq = (const u16*)d_in[1];
  const u16* wk = (const u16*)d_in[2];
  const u16* wv = (const u16*)d_in[3];
  const u16* wo = (const u16*)d_in[4];
  const u16* w1 = (const u16*)d_in[6];
  const u16* b1 = (const u16*)d_in[7];
  const u16* w2 = (const u16*)d_in[8];
  const u16* b2 = (const u16*)d_in[9];
  const u16* gw = (const u16*)d_in[10];
  char* ws = (char*)d_ws;
  (void)hipFuncSetAttribute((const void*)stage1_kernel, hipFuncAttributeMaxDynamicSharedMemorySize, S1_LDS);
  (void)hipFuncSetAttribute((const void*)stage2_kernel, hipFuncAttributeMaxDynamicSharedMemorySize, S2_LDS);
  stage1_kernel<<<S1_QT + S1_KVT, 512, S1_LDS, stream>>>(x, wq, wk, wv, gw, ws);
  stage2_kernel<<<GM_WGS + AT_WGS, 1024, S2_LDS, stream>>>(w1, b1, wo, w2, b2, ws, (u16*)d_out);
}
```

```cpp
#include <hip/hip_runtime.h>
#include <hip/hip_bf16.h>
#include <stdint.h>

typedef short bf16x8 __attribute__((ext_vector_type(8)));
typedef short bf16x4 __attribute__((ext_vector_type(4)));
typedef float f32x16 __attribute__((ext_vector_type(16)));
typedef float f32x4 __attribute__((ext_vector_type(4)));
typedef float f32x2 __attribute__((ext_vector_type(2)));
typedef __bf16 bfx2 __attribute__((ext_vector_type(2)));
typedef unsigned u32x4 __attribute__((ext_vector_type(4)));
typedef unsigned u32x2 __attribute__((ext_vector_type(2)));
typedef unsigned short u16;
typedef __attribute__((address_space(3))) bf16x4* lds_b4p;

#define DI __device__ __forceinline__
#define MFMA32(a, b, c) __builtin_amdgcn_mfma_f32_32x32x16_bf16((a), (b), (c), 0, 0, 0)
#define MFMA32K8(a, b, c) __builtin_amdgcn_mfma_f32_32x32x8bf16_1k((a), (b), (c), 0, 0, 0)
#define MFMA16(a, b, c) __builtin_amdgcn_mfma_f32_16x16x32_bf16((a), (b), (c), 0, 0, 0)

constexpr int SEQ = 2048, DM = 640, NH = 32, DH = 20, FF = 1536;
constexpr int KVCH = 128;
constexpr int KV_KBYTES = KVCH * 48;
constexpr int KV_VSTR = 704;
constexpr int KV_CHBYTES = 12288;
constexpr int NCH = SEQ / KVCH;

constexpr size_t WS_Q = 0;
constexpr size_t WS_H = WS_Q + (size_t)SEQ * DM * 2;
constexpr size_t WS_O = WS_H + (size_t)SEQ * DM * 2;
constexpr size_t WS_U = WS_O + (size_t)SEQ * DM * 2;
constexpr size_t WS_KV = WS_U + (size_t)FF * DM * 2;
constexpr size_t WS_G = WS_KV + (size_t)NCH * KV_CHBYTES;
constexpr size_t WS_CNT = WS_G + (size_t)SEQ * 4;
constexpr int CNT_U = 0, CNT_O = 5, CNT_N = 69;

DI unsigned pk2(float lo, float hi) {
  f32x2 v = {lo, hi};
  bfx2 b = __builtin_convertvector(v, bfx2);
  return __builtin_bit_cast(unsigned, b);
}
DI float bf2f(u16 v) { return __uint_as_float(((unsigned)v) << 16); }
DI u16 f2bf(float f) { return (u16)(pk2(f, 0.f) & 0xffffu); }

DI void glds16(const void* g, unsigned ldsaddr) {
  unsigned keep;
  asm volatile("s_mov_b32 %0, m0\n\ts_mov_b32 m0, %2\n\ts_nop 0\n\tglobal_load_lds_dwordx4 %1, off\n\ts_mov_b32 m0, %0"
               : "=&s"(keep)
               : "v"(g), "s"(ldsaddr)
               : "memory");
}
DI void glds16_dev(const void* g, unsigned ldsaddr) {
  unsigned keep;
  asm volatile("s_mov_b32 %0, m0\n\ts_mov_b32 m0, %2\n\ts_nop 0\n\tglobal_load_lds_dwordx4 %1, off sc1\n\ts_mov_b32 m0, %0"
               : "=&s"(keep)
               : "v"(g), "s"(ldsaddr)
               : "memory");
}
DI unsigned lds_addr(const void* p) { return (unsigned)(size_t)p; }

#define WAITV(n) asm volatile("s_waitcnt vmcnt(" #n ")" ::: "memory")
#define BARRIER() do { asm volatile("s_waitcnt lgkmcnt(0)" ::: "memory"); __builtin_amdgcn_s_barrier(); asm volatile("" ::: "memory"); } while (0)

DI int fresh_lane() {
  int l;
  asm volatile("v_mbcnt_lo_u32_b32 %0, -1, 0\n\tv_mbcnt_hi_u32_b32 %0, -1, %0" : "=v"(l));
  return l;
}
DI float max_halves(float m) {
  auto r = __builtin_amdgcn_permlane32_swap(__float_as_uint(m), __float_as_uint(m), false, false);
  return fmaxf(__uint_as_float(r[0]), __uint_as_float(r[1]));
}
DI float upper_half(float v) {
  auto r = __builtin_amdgcn_permlane32_swap(__float_as_uint(v), __float_as_uint(v), false, false);
  return __uint_as_float(r[1]);
}
DI bf16x8 tr_pair(const char* p0, const char* p1) {
  bf16x4 lo = __builtin_amdgcn_ds_read_tr16_b64_v4i16((lds_b4p)(p0));
  bf16x4 hi = __builtin_amdgcn_ds_read_tr16_b64_v4i16((lds_b4p)(p1));
  return __builtin_shufflevector(lo, hi, 0, 1, 2, 3, 4, 5, 6, 7);
}

constexpr int GSTG = 18432;
template <int NCT> constexpr int gboff() { return NCT == 5 ? 10240 : 8192; }
template <int NCT> constexpr int gstg() { return gboff<NCT>() + 8192; }
DI int colswz(int r) { return (((r >> 1) & 1) | (((r >> 3) & 1) << 1)) << 1; }

struct GemmSeg {
  const u16* src[5];
  size_t astep, bstep;
  int nk, kcur;
};
template <bool BROW>
DI void seg_init(GemmSeg& sg, const u16* A, int lda, const u16* B, int ldb, int nk, int k0, int tid) {
  sg.astep = (size_t)64 * lda;
  sg.bstep = BROW ? (size_t)64 : (size_t)64 * ldb;
  sg.nk = nk;
  sg.kcur = k0;
  A += (size_t)k0 * sg.astep;
  B += (size_t)k0 * sg.bstep;
#pragma unroll
  for (int i = 0; i < 2; ++i) {
    const int c = tid + 256 * i, r = c >> 3, ch = (c & 7) ^ colswz(r);
    sg.src[i] = A + (size_t)r * lda + ch * 8;
    if (BROW) sg.src[2 + i] = B + (size_t)r * ldb + ((c & 7) ^ ((r >> 1) & 7)) * 8;
    else sg.src[2 + i] = B + (size_t)r * ldb + ch * 8;
  }
  const int pos = (tid & 127) >> 1, r = pos ^ (((pos >> 3) & 1) << 2);
  sg.src[4] = A + (size_t)r * lda + 64 + (tid & 1) * 8;
}
template <int NCT, int DEV>
DI void seg_stage(GemmSeg& sg, unsigned b  , int wave) {
  if (DEV == 1) { glds16_dev(sg.src[0], b); glds16_dev(sg.src[1], b + 4096); }
  else { glds16(sg.src[0], b); glds16(sg.src[1], b + 4096); }
  if (DEV == 2) { glds16_dev(sg.src[2], b + gboff<NCT>()); glds16_dev(sg.src[3], b + gboff<NCT>() + 4096); }
  else { glds16(sg.src[2], b + gboff<NCT>()); glds16(sg.src[3], b + gboff<NCT>() + 4096); }
  if (NCT == 5 && wave < 2) {
    if (DEV == 1) glds16_dev(sg.src[4], b + 8192);
    else glds16(sg.src[4], b + 8192);
  }
  sg.kcur += 1;
  if (sg.kcur == sg.nk) {
    sg.kcur = 0;
    const size_t ab = sg.astep * (sg.nk - 1), bb = sg.bstep * (sg.nk - 1);
    sg.src[0] -= ab; sg.src[1] -= ab; sg.src[4] -= ab;
    sg.src[2] -= bb; sg.src[3] -= bb;
  } else {
    sg.src[0] += sg.astep; sg.src[1] += sg.astep; sg.src[4] += sg.astep;
    sg.src[2] += sg.bstep; sg.src[3] += sg.bstep;
  }
}
struct FragAddr { int aoff, asw, a2off, boffc, boffr, bsw, g; };
DI FragAddr frag_addr(int wave, int lane) {
  FragAddr f;
  const int g = lane >> 4, q = (lane & 15) >> 2, p = lane & 3;
  const int kr = 8 * g + q;
  f.g = g;
  f.aoff = kr * 128 + 8 * p;
  f.asw = colswz(kr);
  f.a2off = 8192 + (kr ^ (((kr >> 3) & 1) << 2)) * 32 + 8 * p;
  const int row = 16 * wave + (lane & 15);
  f.boffr = row * 128;
  f.boffc = kr * 128 + (((2 * wave) ^ f.asw) << 4) + 8 * p;
  f.bsw = (row >> 1) & 7;
  return f;
}
template <bool BROW, int NCT>
DI void gemm_step(f32x4 (&acc)[5], const char* base, const FragAddr& f) {
#pragma unroll
  for (int ks = 0; ks < 2; ++ks) {
    bf16x8 fa[5], fb;
#pragma unroll
    for (int ct = 0; ct < 4; ++ct) {
      const char* a = base + ks * 4096 + f.aoff + (((2 * ct) ^ f.asw) << 4);
      fa[ct] = tr_pair(a, a + 512);
    }
    if (NCT == 5) fa[4] = tr_pair(base + ks * 1024 + f.a2off, base + ks * 1024 + (f.a2off ^ 128));
    const char* bb = base + gboff<NCT>();
    if (BROW) fb = *(const bf16x8*)(bb + f.boffr + (((4 * ks + f.g) ^ f.bsw) << 4));
    else fb = tr_pair(bb + ks * 4096 + f.boffc, bb + ks * 4096 + f.boffc + 512);
#pragma unroll
    for (int ct = 0; ct < NCT; ++ct) acc[ct] = MFMA16(fa[ct], fb, acc[ct]);
  }
}
template <int NCT, int N>
DI void wait_stages(int wave) {
  if (NCT == 5 && wave < 2) asm volatile("s_waitcnt vmcnt(%0)" ::"i"(5 * N) : "memory");
  else asm volatile("s_waitcnt vmcnt(%0)" ::"i"(4 * N) : "memory");
}
template <int NCT, int N>
DI void loader_tail(int lw) {
  wait_stages<NCT, N>(lw);
  __builtin_amdgcn_s_barrier();
  if constexpr (N > 0) loader_tail<NCT, N - 1>(lw);
}
typedef __attribute__((address_space(1))) unsigned gu32;
typedef __attribute__((address_space(1))) unsigned long long gu64;
DI void wait_count(const char* cnt, unsigned target) {
  gu32* p = (gu32*)cnt;
  for (unsigned spins = 0; spins < (1u << 22); ++spins) {
    if ((unsigned)__builtin_amdgcn_readfirstlane(__hip_atomic_load(p, __ATOMIC_RELAXED, __HIP_MEMORY_SCOPE_AGENT)) >= target)
      break;
    __builtin_amdgcn_s_sleep(8);
  }
  __builtin_amdgcn_fence(__ATOMIC_ACQUIRE, "wavefront");
}
DI void store8_dev(void* p, u32x2 v) {
  __hip_atomic_store((gu64*)p, ((unsigned long long)v[1] << 32) | v[0], __ATOMIC_RELAXED, __HIP_MEMORY_SCOPE_AGENT);
}
template <int NCT, int NST, int DEV0, int DEV1>
DI void gemm_loader(GemmSeg& s0, int n0, GemmSeg& s1, int n1, char* lds, int lw, const char* cnt1 = nullptr,
                    unsigned tgt1 = 0) {
  const unsigned lbase = lds_addr(lds) + lw * 1024;
#pragma unroll
  for (int i = 0; i < NST - 1; ++i) seg_stage<NCT, DEV0>(s0, lbase + i * gstg<NCT>(), lw);
  int nb = NST - 1;
  for (int nx = NST - 1; nx < n0; ++nx) {
    wait_stages<NCT, NST - 2>(lw);
    __builtin_amdgcn_s_barrier();
    seg_stage<NCT, DEV0>(s0, lbase + nb * gstg<NCT>(), lw);
    nb = nb == NST - 1 ? 0 : nb + 1;
  }
  if (cnt1 != nullptr) {
    loader_tail<NCT, NST - 2>(lw);
    wait_count(cnt1, tgt1);
#pragma unroll
    for (int i = 0; i < NST - 2; ++i) {
      seg_stage<NCT, DEV1>(s1, lbase + nb * gstg<NCT>(), lw);
      nb = nb == NST - 1 ? 0 : nb + 1;
    }
    for (int nx = NST - 2; nx < n1; ++nx) {
      wait_stages<NCT, NST - 3>(lw);
      __builtin_amdgcn_s_barrier();
      seg_stage<NCT, DEV1>(s1, lbase + nb * gstg<NCT>(), lw);
      nb = nb == NST - 1 ? 0 : nb + 1;
    }
    loader_tail<NCT, NST - 3>(lw);
    return;
  }
  for (int nx = 0; nx < n1; ++nx) {
    wait_stages<NCT, NST - 2>(lw);
    __builtin_amdgcn_s_barrier();
    seg_stage<NCT, DEV1>(s1, lbase + nb * gstg<NCT>(), lw);
    nb = nb == NST - 1 ? 0 : nb + 1;
  }
  loader_tail<NCT, NST - 2>(lw);
}
template <bool BROW, int NCT, int NST>
DI void gemm_consume(f32x4 (&acc)[5], int nk, int& buf, char* lds, const FragAddr& f) {
  for (int kt = 0; kt < nk; ++kt) {
    BARRIER();
    gemm_step<BROW, NCT>(acc, lds + buf * gstg<NCT>(), f);
    buf = buf == NST - 1 ? 0 : buf + 1;
  }
}

constexpr int S1_NST = 4;
constexpr int S1_QT = 256;
constexpr int S1_KVT = 2 * (SEQ / 32);
constexpr int S1_LDS = S1_NST * GSTG;

__global__ __launch_bounds__(512) void stage1_kernel(const u16* __restrict__ x, const u16* __restrict__ wq,
                                                     const u16* __restrict__ wk, const u16* __restrict__ wv,
                                                     const u16* __restrict__ gw, char* __restrict__ ws) {
  extern __shared__ __attribute__((aligned(16))) char lds[];
  const int tid = threadIdx.x, lane = tid & 63;
  const int wave = __builtin_amdgcn_readfirstlane(tid >> 6);
  int bid = blockIdx.x;
  if (bid == 0 && tid < CNT_N) *(unsigned*)(ws + WS_CNT + tid * 128) = 0u;
  if (bid >= S1_KVT) {
    bid -= S1_KVT;
    const int xcd = bid & 7, j = bid >> 3;
    const int ts = xcd * 4 + (j >> 3), tn = j & 7;
    const int n0 = tn * 80, s0 = ts * 64;
    if (wave >= 4) {
      GemmSeg sg;
      seg_init<true>(sg, wq + n0, DM, x + (size_t)s0 * DM, DM, DM / 64, (tn + 3 * (j >> 3)) % 10, tid - 256);
      gemm_loader<5, S1_NST, 0, 0>(sg, DM / 64, sg, 0, lds, wave - 4);
      return;
    }
    f32x4 acc[5] = {};
    {
      const FragAddr f = frag_addr(wave, lane);
      int buf = 0;
      gemm_consume<true, 5, S1_NST>(acc, DM / 64, buf, lds, f);
    }
    const float qs = 1.4426950408889634f * 0.22360679774997896f;
    u16* q = (u16*)(ws + WS_Q);
    const int s = s0 + 16 * wave + (lane & 15);
#pragma unroll
    for (int ct = 0; ct < 5; ++ct) {
      u32x2 v = {pk2(acc[ct][0] * qs, acc[ct][1] * qs), pk2(acc[ct][2] * qs, acc[ct][3] * qs)};
      *(u32x2*)(q + (size_t)s * DM + n0 + 16 * ct + 4 * (lane >> 4)) = v;
    }
    return;
  }
  {
    const int isv = bid & 1, s0 = (bid >> 1) * 32;
    char* ximg = lds;
    char* wimg = lds + 40960;
    {
      const u16* w = isv ? wv : wk;
      for (int i = wave; i < 65; i += 8) {
        if (i < 40) {
          const int kt = i >> 2, row = (i & 3) * 8 + (lane >> 3);
          const int c = (lane & 7) ^ ((row >> 1) & 7);
          glds16(x + (size_t)(s0 + row) * DM + kt * 64 + c * 8, lds_addr(ximg) + i * 1024);
        } else {
          glds16((const char*)w + (i - 40) * 1024 + lane * 16, lds_addr(wimg) + (i - 40) * 1024);
        }
      }
    }
    WAITV(0);
    BARRIER();
    const int hh = lane >> 5;
    if (wave < 4) {
      const int i16 = lane & 15, q4 = i16 >> 2, p4 = i16 & 3, g1 = (lane >> 4) & 1;
      const int row = lane & 31;
      f32x16 acc = {};
#pragma unroll
      for (int i = 0; i < 10; ++i) {
        const int k16 = wave * 10 + i;
        const int krow = 16 * k16 + 8 * hh + q4;
        const char* ap = wimg + krow * 40 + (16 * g1 + 4 * p4) * 2;
        bf16x8 fa = tr_pair(ap, ap + 4 * 40);
        const int c = 2 * (k16 & 3) + hh;
        bf16x8 fb = *(const bf16x8*)(ximg + (k16 >> 2) * 4096 + row * 128 + ((c ^ ((row >> 1) & 7)) << 4));
        acc = MFMA32(fa, fb, acc);
      }
      float* red = (float*)(lds + 40960 + 26624);
      (void)red;
      BARRIER();
      float* rb = (float*)lds;
      if (wave > 0) {
#pragma unroll
        for (int i = 0; i < 12; ++i) rb[((wave - 1) * 12 + i) * 64 + lane] = acc[i];
      }
      BARRIER();
      if (wave == 0) {
#pragma unroll
        for (int i = 0; i < 12; ++i) acc[i] += rb[i * 64 + lane] + rb[(12 + i) * 64 + lane] + rb[(24 + i) * 64 + lane];
        const int s = s0 + row;
        char* kvp = ws + WS_KV + (size_t)(s / KVCH) * KV_CHBYTES;
        const int key = s % KVCH;
        const int kb = key >> 4, r = key & 15, vh = (r >> 2) & 1, vj = (r & 3) + 4 * (r >> 3);
        char* vbase = kvp + KV_KBYTES + kb * KV_VSTR + vh * 352 + vj * 2;
#pragma unroll
        for (int g = 0; g < 3; ++g) {
          const int c0 = 8 * g + 4 * hh;
          if (c0 < 20) {
            if (!isv) {
              u32x2 v = {pk2(acc[4 * g], acc[4 * g + 1]), pk2(acc[4 * g + 2], acc[4 * g + 3])};
              *(u32x2*)(kvp + key * 48 + c0 * 2) = v;
            } else {
#pragma unroll
              for (int j = 0; j < 4; ++j) *(u16*)(vbase + (c0 + j) * 16) = f2bf(acc[4 * g + j]);
            }
          }
        }
        if (hh == 1) {
          if (!isv) {
            u32x2 v = {0x00003f80u, 0u};
            *(u32x2*)(kvp + key * 48 + 40) = v;
          } else {
            *(u16*)(vbase + 20 * 16) = 0x3f80;
            *(u16*)(vbase + 21 * 16) = 0;
          }
        }
      }
    } else {
      u16* h = (u16*)(ws + WS_H);
      float* gate = (float*)(ws + WS_G);
      const bool two = lane < 16;
      const u32x4 g0 = *(const u32x4*)(gw + lane * 8);
      u32x4 g1 = {0u, 0u, 0u, 0u};
      if (two) g1 = *(const u32x4*)(gw + 512 + lane * 8);
      u32x4 xa[4], xb[4];
#pragma unroll
      for (int rr = 0; rr < 4; ++rr) {
        const int row = isv * 16 + (wave - 4) * 4 + rr;
        const int sw = (row >> 1) & 7;
        xa[rr] = *(const u32x4*)(ximg + (lane >> 3) * 4096 + row * 128 + (((lane & 7) ^ sw) << 4));
        xb[rr] = u32x4{0u, 0u, 0u, 0u};
        if (two) xb[rr] = *(const u32x4*)(ximg + (8 + (lane >> 3)) * 4096 + row * 128 + (((lane & 7) ^ sw) << 4));
      }
      BARRIER();
      BARRIER();
      float sum[4], dot[4], var[4];
#pragma unroll
      for (int rr = 0; rr < 4; ++rr) {
        float sm = 0.f, dt = 0.f;
#pragma unroll
        for (int i = 0; i < 4; ++i) {
          const float a0 = __uint_as_float(xa[rr][i] << 16), a1 = __uint_as_float(xa[rr][i] & 0xffff0000u);
          const float b0 = __uint_as_float(xb[rr][i] << 16), b1 = __uint_as_float(xb[rr][i] & 0xffff0000u);
          sm += (a0 + a1) + (b0 + b1);
          dt += a0 * __uint_as_float(g0[i] << 16) + a1 * __uint_as_float(g0[i] & 0xffff0000u);
          dt += b0 * __uint_as_float(g1[i] << 16) + b1 * __uint_as_float(g1[i] & 0xffff0000u);
        }
        sum[rr] = sm; dot[rr] = dt;
      }
#pragma unroll
      for (int o = 32; o >= 1; o >>= 1) {
#pragma unroll
        for (int rr = 0; rr < 4; ++rr) { sum[rr] += __shfl_xor(sum[rr], o); dot[rr] += __shfl_xor(dot[rr], o); }
      }
#pragma unroll
      for (int rr = 0; rr < 4; ++rr) {
        const float mean = sum[rr] * (1.f / DM);
        sum[rr] = mean;
        float vr = 0.f;
#pragma unroll
        for (int i = 0; i < 4; ++i) {
          const float a0 = __uint_as_float(xa[rr][i] << 16) - mean, a1 = __uint_as_float(xa[rr][i] & 0xffff0000u) - mean;
          const float b0 = __uint_as_float(xb[rr][i] << 16) - mean, b1 = __uint_as_float(xb[rr][i] & 0xffff0000u) - mean;
          vr += a0 * a0 + a1 * a1;
          if (two) vr += b0 * b0 + b1 * b1;
        }
        var[rr] = vr;
      }
#pragma unroll
      for (int o = 32; o >= 1; o >>= 1) {
#pragma unroll
        for (int rr = 0; rr < 4; ++rr) var[rr] += __shfl_xor(var[rr], o);
      }
#pragma unroll
      for (int rr = 0; rr < 4; ++rr) {
        const int s = s0 + isv * 16 + (wave - 4) * 4 + rr;
        const float mean = sum[rr];
        const float rstd = 1.f / sqrtf(var[rr] * (1.f / DM) + 1e-5f);
        u32x4 o0, o1;
#pragma unroll
        for (int i = 0; i < 4; ++i) {
          o0[i] = pk2((__uint_as_float(xa[rr][i] << 16) - mean) * rstd, (__uint_as_float(xa[rr][i] & 0xffff0000u) - mean) * rstd);
          o1[i] = pk2((__uint_as_float(xb[rr][i] << 16) - mean) * rstd, (__uint_as_float(xb[rr][i] & 0xffff0000u) - mean) * rstd);
        }
        *(u32x4*)(h + (size_t)s * DM + lane * 8) = o0;
        if (two) *(u32x4*)(h + (size_t)s * DM + 512 + lane * 8) = o1;
        if (lane == 0) gate[s] = 1.f / (1.f + __expf(-dot[rr]));
      }
    }
  }
}

constexpr int H_STG = 24576;
struct Seg32 {
  const u16* a;
  const u16* b;
  size_t astep, bstep;
  int lda, ldb, nk, kcur;
};
template <bool BROW>
DI void seg32_init(Seg32& sg, const u16* A, int lda, const u16* B, int ldb, int nk, int k0, int lane) {
  sg.astep = (size_t)64 * lda;
  sg.bstep = BROW ? (size_t)64 : (size_t)64 * ldb;
  sg.lda = lda; sg.ldb = ldb; sg.nk = nk; sg.kcur = k0;
  sg.a = A + (size_t)k0 * sg.astep + (size_t)(lane >> 2) * lda + (lane & 3) * 8;
  if (BROW) sg.b = B + (size_t)k0 * sg.bstep + (size_t)(lane >> 3) * ldb;
  else sg.b = B + (size_t)k0 * sg.bstep + (size_t)(lane >> 2) * ldb + (lane & 3) * 8;
}
template <int NCT, bool BROW, int DEV>
DI void seg32_stage(Seg32& sg, unsigned lbuf, int lw, int lane) {
  constexpr int NP = 4 * NCT + 4;
#pragma unroll
  for (int i = 0; i < 8; ++i) {
    const int p = lw + 3 * i;
    if (p < 4 * NCT) {
      const u16* g = sg.a + (size_t)((p & 3) * 16) * sg.lda + (p >> 2) * 32;
      if (DEV == 1) glds16_dev(g, lbuf + p * 1024); else glds16(g, lbuf + p * 1024);
    } else if (p < NP) {
      const int pb = p - 4 * NCT;
      const u16* g;
      if (BROW) {
        const int row = pb * 8 + (lane >> 3);
        g = sg.b + (size_t)(pb * 8) * sg.ldb + (((lane & 7) ^ ((row >> 1) & 7)) * 8);
      } else {
        g = sg.b + (size_t)(pb * 16) * sg.ldb;
      }
      if (DEV == 2) glds16_dev(g, lbuf + p * 1024); else glds16(g, lbuf + p * 1024);
    }
  }
  sg.kcur += 1;
  if (sg.kcur == sg.nk) {
    sg.kcur = 0;
    sg.a -= sg.astep * (sg.nk - 1);
    sg.b -= sg.bstep * (sg.nk - 1);
  } else {
    sg.a += sg.astep;
    sg.b += sg.bstep;
  }
}
template <int NCT, int N>
DI void wait32(int lw) {
  if (NCT == 5) asm volatile("s_waitcnt vmcnt(%0)" ::"i"(8 * N) : "memory");
  else if (lw < 2) asm volatile("s_waitcnt vmcnt(%0)" ::"i"(7 * N) : "memory");
  else asm volatile("s_waitcnt vmcnt(%0)" ::"i"(6 * N) : "memory");
}
template <int NCT, int DEV0, bool TWO>
DI void loader32(Seg32& s0, int n0, Seg32& s1, int n1, char* lds, int lw, int lane, const char* cnt1, unsigned tgt1) {
  const unsigned lb = lds_addr(lds);
  seg32_stage<NCT, false, DEV0>(s0, lb, lw, lane);
  seg32_stage<NCT, false, DEV0>(s0, lb + H_STG, lw, lane);
  int nb = 2;
  for (int nx = 2; nx < n0; ++nx) {
    wait32<NCT, 1>(lw);
    __builtin_amdgcn_s_barrier();
    seg32_stage<NCT, false, DEV0>(s0, lb + nb * H_STG, lw, lane);
    nb = nb == 2 ? 0 : nb + 1;
  }
  wait32<NCT, 1>(lw);
  __builtin_amdgcn_s_barrier();
  wait32<NCT, 0>(lw);
  __builtin_amdgcn_s_barrier();
  if (TWO) {
    wait_count(cnt1, tgt1);
    seg32_stage<NCT, true, 2>(s1, lb + nb * H_STG, lw, lane);
    nb = nb == 2 ? 0 : nb + 1;
    seg32_stage<NCT, true, 2>(s1, lb + nb * H_STG, lw, lane);
    nb = nb == 2 ? 0 : nb + 1;
    for (int nx = 2; nx < n1; ++nx) {
      wait32<NCT, 1>(lw);
      __builtin_amdgcn_s_barrier();
      seg32_stage<NCT, true, 2>(s1, lb + nb * H_STG, lw, lane);
      nb = nb == 2 ? 0 : nb + 1;
    }
    wait32<NCT, 1>(lw);
    __builtin_amdgcn_s_barrier();
    wait32<NCT, 0>(lw);
    __builtin_amdgcn_s_barrier();
  }
}
template <int NCT, bool BROW>
DI void consume32(f32x16& acc, int nk, int& buf, const char* lds, int cw, int lane) {
  const int i16 = lane & 15, q = i16 >> 2, p = i16 & 3, h = lane >> 5, g1 = (lane >> 4) & 1;
  const int coff = (8 * h + q) * 64 + (16 * g1 + 4 * p) * 2;
  const int row = lane & 31, rsw = (row >> 1) & 7;
  for (int kt = 0; kt < nk; ++kt) {
    BARRIER();
    if (cw < NCT) {
      const char* ab = lds + buf * H_STG + cw * 4096 + coff;
      const char* bb = lds + buf * H_STG + NCT * 4096;
      bf16x8 fa[4], fb[4];
#pragma unroll
      for (int kk = 0; kk < 4; ++kk) {
        fa[kk] = tr_pair(ab + kk * 1024, ab + kk * 1024 + 256);
        if (BROW) fb[kk] = *(const bf16x8*)(bb + row * 128 + (((2 * kk + h) ^ rsw) << 4));
        else fb[kk] = tr_pair(bb + coff + kk * 1024, bb + coff + kk * 1024 + 256);
      }
#pragma unroll
      for (int kk = 0; kk < 4; ++kk) acc = MFMA32(fa[kk], fb[kk], acc);
    }
    buf = buf == 2 ? 0 : buf + 1;
  }
}

constexpr int GM_WGS = 256;
constexpr int AT_WGS = SEQ / 8;
constexpr int AT_STG = 2 * KV_CHBYTES;
constexpr int S2_FLAG = 3 * AT_STG;
constexpr int S2_SHIFT = S2_FLAG + 64;
constexpr int S2_LDS = S2_SHIFT + 4096;

template <bool SAFE>
DI void attn_pass(const char* __restrict__ ws, char* lds, int srow, f32x16& o, int wave, int lane) {
  const int hd = lane & 31, hh = lane >> 5;
  const int half = wave >> 3;
  const char* kvg = ws + WS_KV;
  bf16x8 qf0;
  bf16x4 qf1;
  {
    const u16* q = (const u16*)(ws + WS_Q) + (size_t)srow * DM + hd * DH;
    u32x2 a = *(const u32x2*)(q + 8 * hh), b = *(const u32x2*)(q + 8 * hh + 4), c = *(const u32x2*)(q + 16);
    u32x4 t0 = {a[0], a[1], b[0], b[1]};
    u32x2 t1 = {hh == 0 ? c[0] : 0u, hh == 0 ? c[1] : 0u};
    qf0 = __builtin_bit_cast(bf16x8, t0);
    qf1 = __builtin_bit_cast(bf16x4, t1);
  }
  const int koff0 = half * KV_CHBYTES + hd * 48 + 16 * hh, koff1 = half * KV_CHBYTES + hd * 48 + 32 + 8 * hh;
  const int voff = half * KV_CHBYTES + KV_KBYTES + hh * 352 + (hd < 21 ? hd : 21) * 16;
  const int pc0 = wave, pc1 = 16 + wave;
  const char* gsrc0 = kvg + (size_t)((pc0 / 12) * (NCH / 2)) * KV_CHBYTES + (pc0 % 12) * 1024 + lane * 16;
  const int gd1 = (NCH / 2) * KV_CHBYTES + (pc1 - 12) * 1024 - pc0 * 1024;
  const unsigned ldst = lds_addr(lds);
  auto stage = [&](int boff) {
    glds16(gsrc0, ldst + boff + pc0 * 1024);
    if (wave < 8) glds16(gsrc0 + gd1, ldst + boff + pc1 * 1024);
    gsrc0 += KV_CHBYTES;
  };
  float mrun = -3.0e38f;
  stage(0);
  stage(AT_STG);
  if (wave < 8) WAITV(2); else WAITV(1);
  BARRIER();
  int bcur = 0;
  bf16x8 k0 = *(const bf16x8*)(lds + koff0);
  bf16x4 k1 = *(const bf16x4*)(lds + koff1);
  bf16x8 v0 = *(const bf16x8*)(lds + voff);
  bf16x8 v1 = *(const bf16x8*)(lds + voff + KV_VSTR);
  if (!SAFE) {
    f32x16 z = {};
    f32x16 s = MFMA32(k0, qf0, z);
    s = MFMA32K8(k1, qf1, s);
    float m = s[0];
#pragma unroll
    for (int i = 1; i < 16; ++i) m = fmaxf(m, s[i]);
    m = max_halves(m);
    const u16 mb = f2bf(m);
    *(float*)(lds + S2_SHIFT + (wave * 64 + lane) * 4) = bf2f(mb);
    if (hh == 1) qf1[0] = (short)(mb ^ 0x8000u);
  }
#pragma unroll 1
  for (int ch = 0; ch < NCH / 2; ++ch) {
    const char* base = lds + bcur;
    const int bnext = bcur == 2 * AT_STG ? 0 : bcur + AT_STG;
#pragma unroll SAFE ? 1 : 4
    for (int t = 0; t < KVCH / 32; ++t) {
      f32x16 z = {};
      f32x16 s = MFMA32(k0, qf0, z);
      s = MFMA32K8(k1, qf1, s);
      bf16x8 cv0 = v0, cv1 = v1;
      if (t + 1 < KVCH / 32) {
        k0 = *(const bf16x8*)(base + (t + 1) * 32 * 48 + koff0);
        k1 = *(const bf16x4*)(base + (t + 1) * 32 * 48 + koff1);
        v0 = *(const bf16x8*)(base + voff + (2 * t + 2) * KV_VSTR);
        v1 = *(const bf16x8*)(base + voff + (2 * t + 3) * KV_VSTR);
      } else {
        WAITV(0);
        BARRIER();
        stage(bnext == 2 * AT_STG ? 0 : bnext + AT_STG);
        const char* nb = lds + bnext;
        k0 = *(const bf16x8*)(nb + koff0);
        k1 = *(const bf16x4*)(nb + koff1);
        v0 = *(const bf16x8*)(nb + voff);
        v1 = *(const bf16x8*)(nb + voff + KV_VSTR);
      }
      if (SAFE) {
        float m = s[0];
#pragma unroll
        for (int i = 1; i < 16; ++i) m = fmaxf(m, s[i]);
        m = max_halves(m);
        const float mn = fmaxf(mrun, m);
        const float alpha = __builtin_amdgcn_exp2f(mrun - mn);
        mrun = mn;
#pragma unroll
        for (int i = 0; i < 16; ++i) { o[i] *= alpha; s[i] -= mn; }
      }
      u32x4 p0, p1;
#pragma unroll
      for (int i = 0; i < 4; ++i) {
        p0[i] = pk2(__builtin_amdgcn_exp2f(s[2 * i]), __builtin_amdgcn_exp2f(s[2 * i + 1]));
        p1[i] = pk2(__builtin_amdgcn_exp2f(s[8 + 2 * i]), __builtin_amdgcn_exp2f(s[8 + 2 * i + 1]));
      }
      o = MFMA32(cv0, __builtin_bit_cast(bf16x8, p0), o);
      o = MFMA32(cv1, __builtin_bit_cast(bf16x8, p1), o);
    }
    bcur = bnext;
  }
  WAITV(0);
  if (SAFE) *(float*)(lds + S2_SHIFT + (wave * 64 + lane) * 4) = mrun;
}

__global__ __launch_bounds__(1024, 6) void stage2_kernel(const u16* __restrict__ w1, const u16* __restrict__ b1,
                                                         const u16* __restrict__ wo, const u16* __restrict__ w2,
                                                         const u16* __restrict__ b2, char* __restrict__ ws,
                                                         u16* __restrict__ out) {
  extern __shared__ __attribute__((aligned(16))) char lds[];
  const int tid = threadIdx.x, lane = tid & 63;
  const int wave = __builtin_amdgcn_readfirstlane(tid >> 6);
  if (blockIdx.x < GM_WGS) {
    if (wave >= 8) return;
    const int bid = blockIdx.x;
    const int xcd = bid & 7, j = bid >> 3;
    const char* cnt = ws + WS_CNT;
    u16* u = (u16*)(ws + WS_U);
    const int tf = xcd * 6 + j / 5, t1 = j % 5;
    const int ts = xcd * 8 + (j >> 2), td = j & 3;
    const int d0 = td * 160, s0 = ts * 32;
    if (wave >= 5) {
      const int lw = wave - 5;
      if (j < 30) {
        Seg32 sg;
        seg32_init<false>(sg, (const u16*)(ws + WS_H) + t1 * 128, DM, w1 + tf * 32, FF, SEQ / 64, (t1 * 6 + j / 5) & 31, lane);
        loader32<4, 0, false>(sg, SEQ / 64, sg, 0, lds, lw, lane, nullptr, 0);
        __builtin_amdgcn_s_barrier();
      }
      wait_count(cnt + (CNT_U + d0 / 128) * 128, 48);
      wait_count(cnt + (CNT_U + (d0 + 159) / 128) * 128, 48);
      Seg32 sb, sa;
      seg32_init<false>(sb, u + d0, DM, w2 + s0, SEQ, FF / 64, (td * 6 + (j >> 2)) % 24, lane);
      seg32_init<true>(sa, wo + d0, DM, (const u16*)(ws + WS_O) + (size_t)s0 * DM, DM, DM / 64, (td * 2 + (j >> 2)) % 10, lane);
      loader32<5, 1, true>(sb, FF / 64, sa, DM / 64, lds, lw, lane, cnt + (CNT_O + ts) * 128, 4);
      return;
    }
    const int hh = lane >> 5;
    if (j < 30) {
      const int fcol = tf * 32 + (lane & 31);
      const float bias = bf2f(b1[fcol]);
      f32x16 acc = {};
      int buf = 0;
      consume32<4, false>(acc, SEQ / 64, buf, lds, wave, lane);
      if (wave < 4) {
#pragma unroll
        for (int g = 0; g < 4; ++g) {
          float r[4];
#pragma unroll
          for (int i = 0; i < 4; ++i) {
            const float t = acc[4 * g + i] + bias;
            const float z = 0.7978845608028654f * (t + 0.044715f * t * t * t);
            r[i] = t / (1.f + __expf(-2.f * z));
          }
          u32x2 v = {pk2(r[0], r[1]), pk2(r[2], r[3])};
          store8_dev(u + (size_t)fcol * DM + t1 * 128 + 32 * wave + 8 * g + 4 * hh, v);
        }
      }
      WAITV(0);
      BARRIER();
      if (tid == 0)
        __hip_atomic_fetch_add((gu32*)(cnt + (CNT_U + t1) * 128), 1u, __ATOMIC_RELAXED, __HIP_MEMORY_SCOPE_AGENT);
    }
    const int s = s0 + (lane & 31);
    const float g = ((const float*)(ws + WS_G))[s], bias = bf2f(b2[s]);
    f32x16 acc = {};
    int buf = 0;
    consume32<5, false>(acc, FF / 64, buf, lds, wave, lane);
#pragma unroll
    for (int i = 0; i < 16; ++i) acc[i] = (acc[i] + bias) * (1.f - g);
    consume32<5, true>(acc, DM / 64, buf, lds, wave, lane);
#pragma unroll
    for (int gg = 0; gg < 4; ++gg) {
      u32x2 v = {pk2(acc[4 * gg], acc[4 * gg + 1]), pk2(acc[4 * gg + 2], acc[4 * gg + 3])};
      *(u32x2*)(out + (size_t)s * DM + d0 + 32 * wave + 8 * gg + 4 * hh) = v;
    }
    return;
  }
  const int half = wave >> 3;
  const int s = (blockIdx.x - GM_WGS) * 8 + (wave & 7);
  f32x16 o = {};
  volatile unsigned* flag = (volatile unsigned*)(lds + S2_FLAG);
  if (tid == 0) *flag = 0u;
  attn_pass<false>(ws, lds, s, o, wave, lane);

  {
    const float l = upper_half(o[8]);
    const bool bad = !(l > 8.67e-19f && l < 1.15e18f);
    if (bad) *flag = 1u;
    __syncthreads();
    if (__builtin_amdgcn_readfirstlane(*flag) != 0u) {
      o = f32x16{};
      attn_pass<true>(ws, lds, s, o, wave, fresh_lane());
    }
  }
  const int lane2 = fresh_lane();
  const int hd = lane2 & 31, hh = lane2 >> 5;
  __syncthreads();
  float* mg = (float*)lds + (wave & 7) * 13 * 64;
  if (half == 1) {
#pragma unroll
    for (int i = 0; i < 12; ++i) mg[i * 64 + lane2] = o[i];
  }
  __syncthreads();
  if (half == 0) {
    const float shift = *(const float*)(lds + S2_SHIFT + (wave * 64 + lane2) * 4);
    const float sh1 = *(const float*)(lds + S2_SHIFT + ((wave + 8) * 64 + lane2) * 4);
    const float mm = fmaxf(shift, sh1);
    const float f0 = __builtin_amdgcn_exp2f(shift - mm), f1 = __builtin_amdgcn_exp2f(sh1 - mm);
#pragma unroll
    for (int i = 0; i < 12; ++i) o[i] = o[i] * f0 + mg[i * 64 + lane2] * f1;
    const float l = upper_half(o[8]);
    const float inv = ((const float*)(ws + WS_G))[s] / l;
    u16* op = (u16*)(ws + WS_O) + (size_t)s * DM + hd * DH + 4 * hh;
#pragma unroll
    for (int g = 0; g < 3; ++g) {
      if (g < 2 || hh == 0) {
        u32x2 v = {pk2(o[4 * g] * inv, o[4 * g + 1] * inv), pk2(o[4 * g + 2] * inv, o[4 * g + 3] * inv)};
        store8_dev(op + 8 * g, v);
      }
    }
    WAITV(0);
  }
  __syncthreads();
  if (wave == 0 && lane2 == 0)
    __hip_atomic_fetch_add((gu32*)(ws + WS_CNT + (CNT_O + (s >> 5)) * 128), 1u, __ATOMIC_RELAXED,
                           __HIP_MEMORY_SCOPE_AGENT);
}

extern "C" void kernel_launch(void* const* d_in, const int* in_sizes, int n_in, void* d_out, int out_size, void* d_ws,
                              size_t ws_size, hipStream_t stream) {
  const u16* x = (const u16*)d_in[0];
  const u16* wq = (const u16*)d_in[1];
  const u16* wk = (const u16*)d_in[2];
  const u16* wv = (const u16*)d_in[3];
  const u16* wo = (const u16*)d_in[4];
  const u16* w1 = (const u16*)d_in[6];
  const u16* b1 = (const u16*)d_in[7];
  const u16* w2 = (const u16*)d_in[8];
  const u16* b2 = (const u16*)d_in[9];
  const u16* gw = (const u16*)d_in[10];
  char* ws = (char*)d_ws;
  (void)hipFuncSetAttribute((const void*)stage1_kernel, hipFuncAttributeMaxDynamicSharedMemorySize, S1_LDS);
  (void)hipFuncSetAttribute((const void*)stage2_kernel, hipFuncAttributeMaxDynamicSharedMemorySize, S2_LDS);
  stage1_kernel<<<S1_QT + S1_KVT, 512, S1_LDS, stream>>>(x, wq, wk, wv, gw, ws);
  stage2_kernel<<<GM_WGS + AT_WGS, 1024, S2_LDS, stream>>>(w1, b1, wo, w2, b2, ws, (u16*)d_out);
}
```

```cpp
#include <hip/hip_runtime.h>
#include <hip/hip_bf16.h>
#include <stdint.h>

typedef short bf16x8 __attribute__((ext_vector_type(8)));
typedef short bf16x4 __attribute__((ext_vector_type(4)));
typedef float f32x16 __attribute__((ext_vector_type(16)));
typedef float f32x4 __attribute__((ext_vector_type(4)));
typedef float f32x2 __attribute__((ext_vector_type(2)));
typedef __bf16 bfx2 __attribute__((ext_vector_type(2)));
typedef unsigned u32x4 __attribute__((ext_vector_type(4)));
typedef unsigned u32x2 __attribute__((ext_vector_type(2)));
typedef unsigned short u16;
typedef __attribute__((address_space(3))) bf16x4* lds_b4p;

#define DI __device__ __forceinline__
#define MFMA32(a, b, c) __builtin_amdgcn_mfma_f32_32x32x16_bf16((a), (b), (c), 0, 0, 0)
#define MFMA32K8(a, b, c) __builtin_amdgcn_mfma_f32_32x32x8bf16_1k((a), (b), (c), 0, 0, 0)
#define MFMA16(a, b, c) __builtin_amdgcn_mfma_f32_16x16x32_bf16((a), (b), (c), 0, 0, 0)

constexpr int SEQ = 2048, DM = 640, NH = 32, DH = 20, FF = 1536;
constexpr int KVCH = 128;
constexpr int KV_KBYTES = KVCH * 48;
constexpr int KV_VSTR = 704;
constexpr int KV_CHBYTES = 12288;
constexpr int NCH = SEQ / KVCH;

constexpr size_t WS_Q = 0;
constexpr size_t WS_H = WS_Q + (size_t)SEQ * DM * 2;
constexpr size_t WS_O = WS_H + (size_t)SEQ * DM * 2;
constexpr size_t WS_U = WS_O + (size_t)SEQ * DM * 2;
constexpr size_t WS_KV = WS_U + (size_t)FF * DM * 2;
constexpr size_t WS_G = WS_KV + (size_t)NCH * KV_CHBYTES;
constexpr size_t WS_CNT = WS_G + (size_t)SEQ * 4;
constexpr int CNT_U = 0, CNT_O = 10, CNT_N = 42;

DI unsigned pk2(float lo, float hi) {
  f32x2 v = {lo, hi};
  bfx2 b = __builtin_convertvector(v, bfx2);
  return __builtin_bit_cast(unsigned, b);
}
DI float bf2f(u16 v) { return __uint_as_float(((unsigned)v) << 16); }
DI u16 f2bf(float f) { return (u16)(pk2(f, 0.f) & 0xffffu); }

DI void glds16(const void* g, unsigned ldsaddr) {
  unsigned keep;
  asm volatile("s_mov_b32 %0, m0\n\ts_mov_b32 m0, %2\n\ts_nop 0\n\tglobal_load_lds_dwordx4 %1, off\n\ts_mov_b32 m0, %0"
               : "=&s"(keep)
               : "v"(g), "s"(ldsaddr)
               : "memory");
}
DI void glds16_dev(const void* g, unsigned ldsaddr) {
  unsigned keep;
  asm volatile("s_mov_b32 %0, m0\n\ts_mov_b32 m0, %2\n\ts_nop 0\n\tglobal_load_lds_dwordx4 %1, off sc1\n\ts_mov_b32 m0, %0"
               : "=&s"(keep)
               : "v"(g), "s"(ldsaddr)
               : "memory");
}
DI unsigned lds_addr(const void* p) { return (unsigned)(size_t)p; }

#define WAITV(n) asm volatile("s_waitcnt vmcnt(" #n ")" ::: "memory")
#define BARRIER() do { asm volatile("s_waitcnt lgkmcnt(0)" ::: "memory"); __builtin_amdgcn_s_barrier(); asm volatile("" ::: "memory"); } while (0)

DI int fresh_lane() {
  int l;
  asm volatile("v_mbcnt_lo_u32_b32 %0, -1, 0\n\tv_mbcnt_hi_u32_b32 %0, -1, %0" : "=v"(l));
  return l;
}
DI float max_halves(float m) {
  auto r = __builtin_amdgcn_permlane32_swap(__float_as_uint(m), __float_as_uint(m), false, false);
  return fmaxf(__uint_as_float(r[0]), __uint_as_float(r[1]));
}
DI float upper_half(float v) {
  auto r = __builtin_amdgcn_permlane32_swap(__float_as_uint(v), __float_as_uint(v), false, false);
  return __uint_as_float(r[1]);
}
DI bf16x8 tr_pair(const char* p0, const char* p1) {
  bf16x4 lo = __builtin_amdgcn_ds_read_tr16_b64_v4i16((lds_b4p)(p0));
  bf16x4 hi = __builtin_amdgcn_ds_read_tr16_b64_v4i16((lds_b4p)(p1));
  return __builtin_shufflevector(lo, hi, 0, 1, 2, 3, 4, 5, 6, 7);
}

constexpr int GSTG = 18432;
template <int NCT> constexpr int gboff() { return NCT == 5 ? 10240 : 8192; }
template <int NCT> constexpr int gstg() { return gboff<NCT>() + 8192; }
DI int colswz(int r) { return (((r >> 1) & 1) | (((r >> 3) & 1) << 1)) << 1; }

struct GemmSeg {
  const u16* src[5];
  size_t astep, bstep;
  int nk, kcur;
};
template <bool BROW>
DI void seg_init(GemmSeg& sg, const u16* A, int lda, const u16* B, int ldb, int nk, int k0, int tid) {
  sg.astep = (size_t)64 * lda;
  sg.bstep = BROW ? (size_t)64 : (size_t)64 * ldb;
  sg.nk = nk;
  sg.kcur = k0;
  A += (size_t)k0 * sg.astep;
  B += (size_t)k0 * sg.bstep;
#pragma unroll
  for (int i = 0; i < 2; ++i) {
    const int c = tid + 256 * i, r = c >> 3, ch = (c & 7) ^ colswz(r);
    sg.src[i] = A + (size_t)r * lda + ch * 8;
    if (BROW) sg.src[2 + i] = B + (size_t)r * ldb + ((c & 7) ^ ((r >> 1) & 7)) * 8;
    else sg.src[2 + i] = B + (size_t)r * ldb + ch * 8;
  }
  const int pos = (tid & 127) >> 1, r = pos ^ (((pos >> 3) & 1) << 2);
  sg.src[4] = A + (size_t)r * lda + 64 + (tid & 1) * 8;
}
template <int NCT, int DEV>
DI void seg_stage(GemmSeg& sg, unsigned b  , int wave) {
  if (DEV == 1) { glds16_dev(sg.src[0], b); glds16_dev(sg.src[1], b + 4096); }
  else { glds16(sg.src[0], b); glds16(sg.src[1], b + 4096); }
  if (DEV == 2) { glds16_dev(sg.src[2], b + gboff<NCT>()); glds16_dev(sg.src[3], b + gboff<NCT>() + 4096); }
  else { glds16(sg.src[2], b + gboff<NCT>()); glds16(sg.src[3], b + gboff<NCT>() + 4096); }
  if (NCT == 5 && wave < 2) {
    if (DEV == 1) glds16_dev(sg.src[4], b + 8192);
    else glds16(sg.src[4], b + 8192);
  }
  sg.kcur += 1;
  if (sg.kcur == sg.nk) {
    sg.kcur = 0;
    const size_t ab = sg.astep * (sg.nk - 1), bb = sg.bstep * (sg.nk - 1);
    sg.src[0] -= ab; sg.src[1] -= ab; sg.src[4] -= ab;
    sg.src[2] -= bb; sg.src[3] -= bb;
  } else {
    sg.src[0] += sg.astep; sg.src[1] += sg.astep; sg.src[4] += sg.astep;
    sg.src[2] += sg.bstep; sg.src[3] += sg.bstep;
  }
}
struct FragAddr { int aoff, asw, a2off, boffc, boffr, bsw, g; };
DI FragAddr frag_addr(int wave, int lane) {
  FragAddr f;
  const int g = lane >> 4, q = (lane & 15) >> 2, p = lane & 3;
  const int kr = 8 * g + q;
  f.g = g;
  f.aoff = kr * 128 + 8 * p;
  f.asw = colswz(kr);
  f.a2off = 8192 + (kr ^ (((kr >> 3) & 1) << 2)) * 32 + 8 * p;
  const int row = 16 * wave + (lane & 15);
  f.boffr = row * 128;
  f.boffc = kr * 128 + (((2 * wave) ^ f.asw) << 4) + 8 * p;
  f.bsw = (row >> 1) & 7;
  return f;
}
template <bool BROW, int NCT>
DI void gemm_step(f32x4 (&acc)[5], const char* base, const FragAddr& f) {
#pragma unroll
  for (int ks = 0; ks < 2; ++ks) {
    bf16x8 fa[5], fb;
#pragma unroll
    for (int ct = 0; ct < 4; ++ct) {
      const char* a = base + ks * 4096 + f.aoff + (((2 * ct) ^ f.asw) << 4);
      fa[ct] = tr_pair(a, a + 512);
    }
    if (NCT == 5) fa[4] = tr_pair(base + ks * 1024 + f.a2off, base + ks * 1024 + (f.a2off ^ 128));
    const char* bb = base + gboff<NCT>();
    if (BROW) fb = *(const bf16x8*)(bb + f.boffr + (((4 * ks + f.g) ^ f.bsw) << 4));
    else fb = tr_pair(bb + ks * 4096 + f.boffc, bb + ks * 4096 + f.boffc + 512);
#pragma unroll
    for (int ct = 0; ct < NCT; ++ct) acc[ct] = MFMA16(fa[ct], fb, acc[ct]);
  }
}
template <int NCT, int N>
DI void wait_stages(int wave) {
  if (NCT == 5 && wave < 2) asm volatile("s_waitcnt vmcnt(%0)" ::"i"(5 * N) : "memory");
  else asm volatile("s_waitcnt vmcnt(%0)" ::"i"(4 * N) : "memory");
}
template <int NCT, int N>
DI void loader_tail(int lw) {
  wait_stages<NCT, N>(lw);
  __builtin_amdgcn_s_barrier();
  if constexpr (N > 0) loader_tail<NCT, N - 1>(lw);
}
typedef __attribute__((address_space(1))) unsigned gu32;
typedef __attribute__((address_space(1))) unsigned long long gu64;
DI void wait_count(const char* cnt, unsigned target) {
  gu32* p = (gu32*)cnt;
  for (unsigned spins = 0; spins < (1u << 22); ++spins) {
    if ((unsigned)__builtin_amdgcn_readfirstlane(__hip_atomic_load(p, __ATOMIC_RELAXED, __HIP_MEMORY_SCOPE_AGENT)) >= target)
      break;
    __builtin_amdgcn_s_sleep(8);
  }
  __builtin_amdgcn_fence(__ATOMIC_ACQUIRE, "wavefront");
}
DI void store8_dev(void* p, u32x2 v) {
  __hip_atomic_store((gu64*)p, ((unsigned long long)v[1] << 32) | v[0], __ATOMIC_RELAXED, __HIP_MEMORY_SCOPE_AGENT);
}
template <int NCT, int NST, int DEV0, int DEV1>
DI void gemm_loader(GemmSeg& s0, int n0, GemmSeg& s1, int n1, char* lds, int lw, const char* cnt1 = nullptr,
                    unsigned tgt1 = 0) {
  const unsigned lbase = lds_addr(lds) + lw * 1024;
#pragma unroll
  for (int i = 0; i < NST - 1; ++i) seg_stage<NCT, DEV0>(s0, lbase + i * gstg<NCT>(), lw);
  int nb = NST - 1;
  for (int nx = NST - 1; nx < n0; ++nx) {
    wait_stages<NCT, NST - 2>(lw);
    __builtin_amdgcn_s_barrier();
    seg_stage<NCT, DEV0>(s0, lbase + nb * gstg<NCT>(), lw);
    nb = nb == NST - 1 ? 0 : nb + 1;
  }
  if (cnt1 != nullptr) {
    loader_tail<NCT, NST - 2>(lw);
    wait_count(cnt1, tgt1);
#pragma unroll
    for (int i = 0; i < NST - 2; ++i) {
      seg_stage<NCT, DEV1>(s1, lbase + nb * gstg<NCT>(), lw);
      nb = nb == NST - 1 ? 0 : nb + 1;
    }
    for (int nx = NST - 2; nx < n1; ++nx) {
      wait_stages<NCT, NST - 3>(lw);
      __builtin_amdgcn_s_barrier();
      seg_stage<NCT, DEV1>(s1, lbase + nb * gstg<NCT>(), lw);
      nb = nb == NST - 1 ? 0 : nb + 1;
    }
    loader_tail<NCT, NST - 3>(lw);
    return;
  }
  for (int nx = 0; nx < n1; ++nx) {
    wait_stages<NCT, NST - 2>(lw);
    __builtin_amdgcn_s_barrier();
    seg_stage<NCT, DEV1>(s1, lbase + nb * gstg<NCT>(), lw);
    nb = nb == NST - 1 ? 0 : nb + 1;
  }
  loader_tail<NCT, NST - 2>(lw);
}
template <bool BROW, int NCT, int NST>
DI void gemm_consume(f32x4 (&acc)[5], int nk, int& buf, char* lds, const FragAddr& f) {
  for (int kt = 0; kt < nk; ++kt) {
    BARRIER();
    gemm_step<BROW, NCT>(acc, lds + buf * gstg<NCT>(), f);
    buf = buf == NST - 1 ? 0 : buf + 1;
  }
}

constexpr int S1_NST = 4;
constexpr int S1_QT = 256;
constexpr int S1_KVT = 2 * (SEQ / 32);
constexpr int S1_LDS = S1_NST * GSTG;

__global__ __launch_bounds__(512) void stage1_kernel(const u16* __restrict__ x, const u16* __restrict__ wq,
                                                     const u16* __restrict__ wk, const u16* __restrict__ wv,
                                                     const u16* __restrict__ gw, char* __restrict__ ws) {
  extern __shared__ __attribute__((aligned(16))) char lds[];
  const int tid = threadIdx.x, lane = tid & 63;
  const int wave = __builtin_amdgcn_readfirstlane(tid >> 6);
  int bid = blockIdx.x;
  if (bid == 0 && tid < CNT_N) *(unsigned*)(ws + WS_CNT + tid * 128) = 0u;
  if (bid >= S1_KVT) {
    bid -= S1_KVT;
    const int xcd = bid & 7, j = bid >> 3;
    const int ts = xcd * 4 + (j >> 3), tn = j & 7;
    const int n0 = tn * 80, s0 = ts * 64;
    if (wave >= 4) {
      GemmSeg sg;
      seg_init<true>(sg, wq + n0, DM, x + (size_t)s0 * DM, DM, DM / 64, (tn + 3 * (j >> 3)) % 10, tid - 256);
      gemm_loader<5, S1_NST, 0, 0>(sg, DM / 64, sg, 0, lds, wave - 4);
      return;
    }
    f32x4 acc[5] = {};
    {
      const FragAddr f = frag_addr(wave, lane);
      int buf = 0;
      gemm_consume<true, 5, S1_NST>(acc, DM / 64, buf, lds, f);
    }
    const float qs = 1.4426950408889634f * 0.22360679774997896f;
    u16* q = (u16*)(ws + WS_Q);
    const int s = s0 + 16 * wave + (lane & 15);
#pragma unroll
    for (int ct = 0; ct < 5; ++ct) {
      u32x2 v = {pk2(acc[ct][0] * qs, acc[ct][1] * qs), pk2(acc[ct][2] * qs, acc[ct][3] * qs)};
      *(u32x2*)(q + (size_t)s * DM + n0 + 16 * ct + 4 * (lane >> 4)) = v;
    }
    return;
  }
  {
    const int isv = bid & 1, s0 = (bid >> 1) * 32;
    char* ximg = lds;
    char* wimg = lds + 40960;
    {
      const u16* w = isv ? wv : wk;
      for (int i = wave; i < 65; i += 8) {
        if (i < 40) {
          const int kt = i >> 2, row = (i & 3) * 8 + (lane >> 3);
          const int c = (lane & 7) ^ ((row >> 1) & 7);
          glds16(x + (size_t)(s0 + row) * DM + kt * 64 + c * 8, lds_addr(ximg) + i * 1024);
        } else {
          glds16((const char*)w + (i - 40) * 1024 + lane * 16, lds_addr(wimg) + (i - 40) * 1024);
        }
      }
    }
    WAITV(0);
    BARRIER();
    const int hh = lane >> 5;
    if (wave < 4) {
      const int i16 = lane & 15, q4 = i16 >> 2, p4 = i16 & 3, g1 = (lane >> 4) & 1;
      const int row = lane & 31;
      f32x16 acc = {};
#pragma unroll
      for (int i = 0; i < 10; ++i) {
        const int k16 = wave * 10 + i;
        const int krow = 16 * k16 + 8 * hh + q4;
        const char* ap = wimg + krow * 40 + (16 * g1 + 4 * p4) * 2;
        bf16x8 fa = tr_pair(ap, ap + 4 * 40);
        const int c = 2 * (k16 & 3) + hh;
        bf16x8 fb = *(const bf16x8*)(ximg + (k16 >> 2) * 4096 + row * 128 + ((c ^ ((row >> 1) & 7)) << 4));
        acc = MFMA32(fa, fb, acc);
      }
      float* red = (float*)(lds + 40960 + 26624);
      (void)red;
      BARRIER();
      float* rb = (float*)lds;
      if (wave > 0) {
#pragma unroll
        for (int i = 0; i < 12; ++i) rb[((wave - 1) * 12 + i) * 64 + lane] = acc[i];
      }
      BARRIER();
      if (wave == 0) {
#pragma unroll
        for (int i = 0; i < 12; ++i) acc[i] += rb[i * 64 + lane] + rb[(12 + i) * 64 + lane] + rb[(24 + i) * 64 + lane];
        const int s = s0 + row;
        char* kvp = ws + WS_KV + (size_t)(s / KVCH) * KV_CHBYTES;
        const int key = s % KVCH;
        const int kb = key >> 4, r = key & 15, vh = (r >> 2) & 1, vj = (r & 3) + 4 * (r >> 3);
        char* vbase = kvp + KV_KBYTES + kb * KV_VSTR + vh * 352 + vj * 2;
#pragma unroll
        for (int g = 0; g < 3; ++g) {
          const int c0 = 8 * g + 4 * hh;
          if (c0 < 20) {
            if (!isv) {
              u32x2 v = {pk2(acc[4 * g], acc[4 * g + 1]), pk2(acc[4 * g + 2], acc[4 * g + 3])};
              *(u32x2*)(kvp + key * 48 + c0 * 2) = v;
            } else {
#pragma unroll
              for (int j = 0; j < 4; ++j) *(u16*)(vbase + (c0 + j) * 16) = f2bf(acc[4 * g + j]);
            }
          }
        }
        if (hh == 1) {
          if (!isv) {
            u32x2 v = {0x00003f80u, 0u};
            *(u32x2*)(kvp + key * 48 + 40) = v;
          } else {
            *(u16*)(vbase + 20 * 16) = 0x3f80;
            *(u16*)(vbase + 21 * 16) = 0;
          }
        }
      }
    } else {
      u16* h = (u16*)(ws + WS_H);
      float* gate = (float*)(ws + WS_G);
      const bool two = lane < 16;
      const u32x4 g0 = *(const u32x4*)(gw + lane * 8);
      u32x4 g1 = {0u, 0u, 0u, 0u};
      if (two) g1 = *(const u32x4*)(gw + 512 + lane * 8);
      u32x4 xa[4], xb[4];
#pragma unroll
      for (int rr = 0; rr < 4; ++rr) {
        const int row = isv * 16 + (wave - 4) * 4 + rr;
        const int sw = (row >> 1) & 7;
        xa[rr] = *(const u32x4*)(ximg + (lane >> 3) * 4096 + row * 128 + (((lane & 7) ^ sw) << 4));
        xb[rr] = u32x4{0u, 0u, 0u, 0u};
        if (two) xb[rr] = *(const u32x4*)(ximg + (8 + (lane >> 3)) * 4096 + row * 128 + (((lane & 7) ^ sw) << 4));
      }
      BARRIER();
      BARRIER();
      float sum[4], dot[4], var[4];
#pragma unroll
      for (int rr = 0; rr < 4; ++rr) {
        float sm = 0.f, dt = 0.f;
#pragma unroll
        for (int i = 0; i < 4; ++i) {
          const float a0 = __uint_as_float(xa[rr][i] << 16), a1 = __uint_as_float(xa[rr][i] & 0xffff0000u);
          const float b0 = __uint_as_float(xb[rr][i] << 16), b1 = __uint_as_float(xb[rr][i] & 0xffff0000u);
          sm += (a0 + a1) + (b0 + b1);
          dt += a0 * __uint_as_float(g0[i] << 16) + a1 * __uint_as_float(g0[i] & 0xffff0000u);
          dt += b0 * __uint_as_float(g1[i] << 16) + b1 * __uint_as_float(g1[i] & 0xffff0000u);
        }
        sum[rr] = sm; dot[rr] = dt;
      }
#pragma unroll
      for (int o = 32; o >= 1; o >>= 1) {
#pragma unroll
        for (int rr = 0; rr < 4; ++rr) { sum[rr] += __shfl_xor(sum[rr], o); dot[rr] += __shfl_xor(dot[rr], o); }
      }
#pragma unroll
      for (int rr = 0; rr < 4; ++rr) {
        const float mean = sum[rr] * (1.f / DM);
        sum[rr] = mean;
        float vr = 0.f;
#pragma unroll
        for (int i = 0; i < 4; ++i) {
          const float a0 = __uint_as_float(xa[rr][i] << 16) - mean, a1 = __uint_as_float(xa[rr][i] & 0xffff0000u) - mean;
          const float b0 = __uint_as_float(xb[rr][i] << 16) - mean, b1 = __uint_as_float(xb[rr][i] & 0xffff0000u) - mean;
          vr += a0 * a0 + a1 * a1;
          if (two) vr += b0 * b0 + b1 * b1;
        }
        var[rr] = vr;
      }
#pragma unroll
      for (int o = 32; o >= 1; o >>= 1) {
#pragma unroll
        for (int rr = 0; rr < 4; ++rr) var[rr] += __shfl_xor(var[rr], o);
      }
#pragma unroll
      for (int rr = 0; rr < 4; ++rr) {
        const int s = s0 + isv * 16 + (wave - 4) * 4 + rr;
        const float mean = sum[rr];
        const float rstd = 1.f / sqrtf(var[rr] * (1.f / DM) + 1e-5f);
        u32x4 o0, o1;
#pragma unroll
        for (int i = 0; i < 4; ++i) {
          o0[i] = pk2((__uint_as_float(xa[rr][i] << 16) - mean) * rstd, (__uint_as_float(xa[rr][i] & 0xffff0000u) - mean) * rstd);
          o1[i] = pk2((__uint_as_float(xb[rr][i] << 16) - mean) * rstd, (__uint_as_float(xb[rr][i] & 0xffff0000u) - mean) * rstd);
        }
        *(u32x4*)(h + (size_t)s * DM + lane * 8) = o0;
        if (two) *(u32x4*)(h + (size_t)s * DM + 512 + lane * 8) = o1;
        if (lane == 0) gate[s] = 1.f / (1.f + __expf(-dot[rr]));
      }
    }
  }
}

constexpr int G1_TILES = 240;
constexpr int GM_WGS = 256;
constexpr int AT_WGS = SEQ / 8;
constexpr int G_NST = 4;
constexpr int AT_STG = 2 * KV_CHBYTES;
constexpr int S2_FLAG = 3 * AT_STG;
constexpr int S2_SHIFT = S2_FLAG + 64;
constexpr int S2_LDS = S2_SHIFT + 4096;

template <bool SAFE>
DI void attn_pass(const char* __restrict__ ws, char* lds, int srow, f32x16& o, int wave, int lane) {
  const int hd = lane & 31, hh = lane >> 5;
  const int half = wave >> 3;
  const char* kvg = ws + WS_KV;
  bf16x8 qf0;
  bf16x4 qf1;
  {
    const u16* q = (const u16*)(ws + WS_Q) + (size_t)srow * DM + hd * DH;
    u32x2 a = *(const u32x2*)(q + 8 * hh), b = *(const u32x2*)(q + 8 * hh + 4), c = *(const u32x2*)(q + 16);
    u32x4 t0 = {a[0], a[1], b[0], b[1]};
    u32x2 t1 = {hh == 0 ? c[0] : 0u, hh == 0 ? c[1] : 0u};
    qf0 = __builtin_bit_cast(bf16x8, t0);
    qf1 = __builtin_bit_cast(bf16x4, t1);
  }
  const int koff0 = half * KV_CHBYTES + hd * 48 + 16 * hh, koff1 = half * KV_CHBYTES + hd * 48 + 32 + 8 * hh;
  const int voff = half * KV_CHBYTES + KV_KBYTES + hh * 352 + (hd < 21 ? hd : 21) * 16;
  const int pc0 = wave, pc1 = 16 + wave;
  const char* gsrc0 = kvg + (size_t)((pc0 / 12) * (NCH / 2)) * KV_CHBYTES + (pc0 % 12) * 1024 + lane * 16;
  const int gd1 = (NCH / 2) * KV_CHBYTES + (pc1 - 12) * 1024 - pc0 * 1024;
  const unsigned ldst = lds_addr(lds);
  auto stage = [&](int boff) {
    glds16(gsrc0, ldst + boff + pc0 * 1024);
    if (wave < 8) glds16(gsrc0 + gd1, ldst + boff + pc1 * 1024);
    gsrc0 += KV_CHBYTES;
  };
  float mrun = -3.0e38f;
  stage(0);
  stage(AT_STG);
  if (wave < 8) WAITV(2); else WAITV(1);
  BARRIER();
  int bcur = 0;
  bf16x8 k0 = *(const bf16x8*)(lds + koff0);
  bf16x4 k1 = *(const bf16x4*)(lds + koff1);
  bf16x8 v0 = *(const bf16x8*)(lds + voff);
  bf16x8 v1 = *(const bf16x8*)(lds + voff + KV_VSTR);
  if (!SAFE) {
    f32x16 z = {};
    f32x16 s = MFMA32(k0, qf0, z);
    s = MFMA32K8(k1, qf1, s);
    float m = s[0];
#pragma unroll
    for (int i = 1; i < 16; ++i) m = fmaxf(m, s[i]);
    m = max_halves(m);
    const u16 mb = f2bf(m);
    *(float*)(lds + S2_SHIFT + (wave * 64 + lane) * 4) = bf2f(mb);
    if (hh == 1) qf1[0] = (short)(mb ^ 0x8000u);
  }
#pragma unroll 1
  for (int ch = 0; ch < NCH / 2; ++ch) {
    const char* base = lds + bcur;
    const int bnext = bcur == 2 * AT_STG ? 0 : bcur + AT_STG;
#pragma unroll SAFE ? 1 : 4
    for (int t = 0; t < KVCH / 32; ++t) {
      f32x16 z = {};
      f32x16 s = MFMA32(k0, qf0, z);
      s = MFMA32K8(k1, qf1, s);
      bf16x8 cv0 = v0, cv1 = v1;
      if (t + 1 < KVCH / 32) {
        k0 = *(const bf16x8*)(base + (t + 1) * 32 * 48 + koff0);
        k1 = *(const bf16x4*)(base + (t + 1) * 32 * 48 + koff1);
        v0 = *(const bf16x8*)(base + voff + (2 * t + 2) * KV_VSTR);
        v1 = *(const bf16x8*)(base + voff + (2 * t + 3) * KV_VSTR);
      } else {
        WAITV(0);
        BARRIER();
        stage(bnext == 2 * AT_STG ? 0 : bnext + AT_STG);
        const char* nb = lds + bnext;
        k0 = *(const bf16x8*)(nb + koff0);
        k1 = *(const bf16x4*)(nb + koff1);
        v0 = *(const bf16x8*)(nb + voff);
        v1 = *(const bf16x8*)(nb + voff + KV_VSTR);
      }
      if (SAFE) {
        float m = s[0];
#pragma unroll
        for (int i = 1; i < 16; ++i) m = fmaxf(m, s[i]);
        m = max_halves(m);
        const float mn = fmaxf(mrun, m);
        const float alpha = __builtin_amdgcn_exp2f(mrun - mn);
        mrun = mn;
#pragma unroll
        for (int i = 0; i < 16; ++i) { o[i] *= alpha; s[i] -= mn; }
      }
      u32x4 p0, p1;
#pragma unroll
      for (int i = 0; i < 4; ++i) {
        p0[i] = pk2(__builtin_amdgcn_exp2f(s[2 * i]), __builtin_amdgcn_exp2f(s[2 * i + 1]));
        p1[i] = pk2(__builtin_amdgcn_exp2f(s[8 + 2 * i]), __builtin_amdgcn_exp2f(s[8 + 2 * i + 1]));
      }
      o = MFMA32(cv0, __builtin_bit_cast(bf16x8, p0), o);
      o = MFMA32(cv1, __builtin_bit_cast(bf16x8, p1), o);
    }
    bcur = bnext;
  }
  WAITV(0);
  if (SAFE) *(float*)(lds + S2_SHIFT + (wave * 64 + lane) * 4) = mrun;
}

__global__ __launch_bounds__(1024, 6) void stage2_kernel(const u16* __restrict__ w1, const u16* __restrict__ b1,
                                                         const u16* __restrict__ wo, const u16* __restrict__ w2,
                                                         const u16* __restrict__ b2, char* __restrict__ ws,
                                                         u16* __restrict__ out) {
  extern __shared__ __attribute__((aligned(16))) char lds[];
  const int tid = threadIdx.x, lane = tid & 63;
  const int wave = __builtin_amdgcn_readfirstlane(tid >> 6);
  if (blockIdx.x < GM_WGS) {
    if (wave >= 8) return;
    const int bid = blockIdx.x;
    const int xcd = bid & 7, j = bid >> 3;
    const char* cnt = ws + WS_CNT;
    u16* u = (u16*)(ws + WS_U);
    const int ts = xcd * 4 + (j >> 3), td = j & 7;
    const int d0 = td * 80, s0 = ts * 64;
    if (wave >= 4) {
      if (j < 30) {
        const int tf = xcd * 3 + j / 10, t1 = j % 10;
        GemmSeg sg;
        seg_init<false>(sg, (const u16*)(ws + WS_H) + t1 * 64, DM, w1 + tf * 64, FF, SEQ / 64, (t1 * 3 + (j / 10)) & 31,
                        tid - 256);
        gemm_loader<4, G_NST, 0, 0>(sg, SEQ / 64, sg, 0, lds, wave - 4);
        __builtin_amdgcn_s_barrier();
      }
      wait_count(cnt + (CNT_U + d0 / 64) * 128, 24);
      wait_count(cnt + (CNT_U + (d0 + 79) / 64) * 128, 24);
      GemmSeg sb, sa;
      seg_init<false>(sb, u + d0, DM, w2 + s0, SEQ, FF / 64, td * 3, tid - 256);
      seg_init<true>(sa, wo + d0, DM, (const u16*)(ws + WS_O) + (size_t)s0 * DM, DM, DM / 64, (td + 3 * (j >> 3)) % 10,
                     tid - 256);
      gemm_loader<5, G_NST, 1, 2>(sb, FF / 64, sa, DM / 64, lds, wave - 4, cnt + (CNT_O + ts) * 128, 8);
      return;
    }
    const FragAddr f = frag_addr(wave, lane);
    if (j < 30) {
      const int tf = xcd * 3 + j / 10, t1 = j % 10;
      const int fcol = tf * 64 + 16 * wave + (lane & 15);
      const float bias = bf2f(b1[fcol]);
      f32x4 acc[5] = {};
      int buf = 0;
      gemm_consume<false, 4, G_NST>(acc, SEQ / 64, buf, lds, f);
#pragma unroll
      for (int ct = 0; ct < 4; ++ct) {
        float r[4];
#pragma unroll
        for (int i = 0; i < 4; ++i) {
          const float t = acc[ct][i] + bias;
          const float z = 0.7978845608028654f * (t + 0.044715f * t * t * t);
          r[i] = t / (1.f + __expf(-2.f * z));
        }
        u32x2 v = {pk2(r[0], r[1]), pk2(r[2], r[3])};
        store8_dev(u + (size_t)fcol * DM + t1 * 64 + 16 * ct + 4 * (lane >> 4), v);
      }
      WAITV(0);
      BARRIER();
      if (tid == 0)
        __hip_atomic_fetch_add((gu32*)(cnt + (CNT_U + t1) * 128), 1u, __ATOMIC_RELAXED, __HIP_MEMORY_SCOPE_AGENT);
    }
    const int s = s0 + 16 * wave + (lane & 15);
    const float g = ((const float*)(ws + WS_G))[s], bias = bf2f(b2[s]);
    f32x4 acc[5] = {};
    {
      int buf = 0;
      gemm_consume<false, 5, G_NST>(acc, FF / 64, buf, lds, f);
#pragma unroll
      for (int ct = 0; ct < 5; ++ct)
#pragma unroll
        for (int i = 0; i < 4; ++i) acc[ct][i] = (acc[ct][i] + bias) * (1.f - g);
      gemm_consume<true, 5, G_NST>(acc, DM / 64, buf, lds, f);
    }
#pragma unroll
    for (int ct = 0; ct < 5; ++ct) {
      u32x2 v = {pk2(acc[ct][0], acc[ct][1]), pk2(acc[ct][2], acc[ct][3])};
      *(u32x2*)(out + (size_t)s * DM + d0 + 16 * ct + 4 * (lane >> 4)) = v;
    }
    return;
  }
  const int half = wave >> 3;
  const int s = (blockIdx.x - GM_WGS) * 8 + (wave & 7);
  f32x16 o = {};
  volatile unsigned* flag = (volatile unsigned*)(lds + S2_FLAG);
  if (tid == 0) *flag = 0u;
  attn_pass<false>(ws, lds, s, o, wave, lane);

  {
    const float l = upper_half(o[8]);
    const bool bad = !(l > 8.67e-19f && l < 1.15e18f);
    if (bad) *flag = 1u;
    __syncthreads();
    if (__builtin_amdgcn_readfirstlane(*flag) != 0u) {
      o = f32x16{};
      attn_pass<true>(ws, lds, s, o, wave, fresh_lane());
    }
  }
  const int lane2 = fresh_lane();
  const int hd = lane2 & 31, hh = lane2 >> 5;
  __syncthreads();
  float* mg = (float*)lds + (wave & 7) * 13 * 64;
  if (half == 1) {
#pragma unroll
    for (int i = 0; i < 12; ++i) mg[i * 64 + lane2] = o[i];
  }
  __syncthreads();
  if (half == 0) {
    const float shift = *(const float*)(lds + S2_SHIFT + (wave * 64 + lane2) * 4);
    const float sh1 = *(const float*)(lds + S2_SHIFT + ((wave + 8) * 64 + lane2) * 4);
    const float mm = fmaxf(shift, sh1);
    const float f0 = __builtin_amdgcn_exp2f(shift - mm), f1 = __builtin_amdgcn_exp2f(sh1 - mm);
#pragma unroll
    for (int i = 0; i < 12; ++i) o[i] = o[i] * f0 + mg[i * 64 + lane2] * f1;
    const float l = upper_half(o[8]);
    const float inv = ((const float*)(ws + WS_G))[s] / l;
    u16* op = (u16*)(ws + WS_O) + (size_t)s * DM + hd * DH + 4 * hh;
#pragma unroll
    for (int g = 0; g < 3; ++g) {
      if (g < 2 || hh == 0) {
        u32x2 v = {pk2(o[4 * g] * inv, o[4 * g + 1] * inv), pk2(o[4 * g + 2] * inv, o[4 * g + 3] * inv)};
        store8_dev(op + 8 * g, v);
      }
    }
    WAITV(0);
  }
  __syncthreads();
  if (wave == 0 && lane2 == 0)
    __hip_atomic_fetch_add((gu32*)(ws + WS_CNT + (CNT_O + (s >> 6)) * 128), 1u, __ATOMIC_RELAXED,
                           __HIP_MEMORY_SCOPE_AGENT);
}

extern "C" void kernel_launch(void* const* d_in, const int* in_sizes, int n_in, void* d_out, int out_size, void* d_ws,
                              size_t ws_size, hipStream_t stream) {
  const u16* x = (const u16*)d_in[0];
  const u16* wq = (const u16*)d_in[1];
  const u16* wk = (const u16*)d_in[2];
  const u16* wv = (const u16*)d_in[3];
  const u16* wo = (const u16*)d_in[4];
  const u16* w1 = (const u16*)d_in[6];
  const u16* b1 = (const u16*)d_in[7];
  const u16* w2 = (const u16*)d_in[8];
  const u16* b2 = (const u16*)d_in[9];
  const u16* gw = (const u16*)d_in[10];
  char* ws = (char*)d_ws;
  (void)hipFuncSetAttribute((const void*)stage1_kernel, hipFuncAttributeMaxDynamicSharedMemorySize, S1_LDS);
  (void)hipFuncSetAttribute((const void*)stage2_kernel, hipFuncAttributeMaxDynamicSharedMemorySize, S2_LDS);
  stage1_kernel<<<S1_QT + S1_KVT, 512, S1_LDS, stream>>>(x, wq, wk, wv, gw, ws);
  stage2_kernel<<<GM_WGS + AT_WGS, 1024, S2_LDS, stream>>>(w1, b1, wo, w2, b2, ws, (u16*)d_out);
}
```

```cpp
#include <hip/hip_runtime.h>
#include <hip/hip_bf16.h>
#include <stdint.h>

typedef short bf16x8 __attribute__((ext_vector_type(8)));
typedef short bf16x4 __attribute__((ext_vector_type(4)));
typedef float f32x16 __attribute__((ext_vector_type(16)));
typedef float f32x4 __attribute__((ext_vector_type(4)));
typedef float f32x2 __attribute__((ext_vector_type(2)));
typedef __bf16 bfx2 __attribute__((ext_vector_type(2)));
typedef unsigned u32x4 __attribute__((ext_vector_type(4)));
typedef unsigned u32x2 __attribute__((ext_vector_type(2)));
typedef unsigned short u16;
typedef __attribute__((address_space(3))) bf16x4* lds_b4p;

#define DI __device__ __forceinline__
#define MFMA32(a, b, c) __builtin_amdgcn_mfma_f32_32x32x16_bf16((a), (b), (c), 0, 0, 0)
#define MFMA32K8(a, b, c) __builtin_amdgcn_mfma_f32_32x32x8bf16_1k((a), (b), (c), 0, 0, 0)
#define MFMA16(a, b, c) __builtin_amdgcn_mfma_f32_16x16x32_bf16((a), (b), (c), 0, 0, 0)

constexpr int SEQ = 2048, DM = 640, NH = 32, DH = 20, FF = 1536;
constexpr int KVCH = 128;
constexpr int KV_KBYTES = KVCH * 48;
constexpr int KV_VSTR = 704;
constexpr int KV_CHBYTES = 12288;
constexpr int NCH = SEQ / KVCH;

constexpr size_t WS_Q = 0;
constexpr size_t WS_H = WS_Q + (size_t)SEQ * DM * 2;
constexpr size_t WS_O = WS_H + (size_t)SEQ * DM * 2;
constexpr size_t WS_U = WS_O + (size_t)SEQ * DM * 2;
constexpr size_t WS_KV = WS_U + (size_t)FF * DM * 2;
constexpr size_t WS_G = WS_KV + (size_t)NCH * KV_CHBYTES;
constexpr size_t WS_CNT = WS_G + (size_t)SEQ * 4;
constexpr int CNT_U = 0, CNT_O = 10, CNT_N = 42;

DI unsigned pk2(float lo, float hi) {
  f32x2 v = {lo, hi};
  bfx2 b = __builtin_convertvector(v, bfx2);
  return __builtin_bit_cast(unsigned, b);
}
DI float bf2f(u16 v) { return __uint_as_float(((unsigned)v) << 16); }
DI u16 f2bf(float f) { return (u16)(pk2(f, 0.f) & 0xffffu); }

DI void glds16(const void* g, unsigned ldsaddr) {
  unsigned keep;
  asm volatile("s_mov_b32 %0, m0\n\ts_mov_b32 m0, %2\n\ts_nop 0\n\tglobal_load_lds_dwordx4 %1, off\n\ts_mov_b32 m0, %0"
               : "=&s"(keep)
               : "v"(g), "s"(ldsaddr)
               : "memory");
}
DI void glds16_dev(const void* g, unsigned ldsaddr) {
  unsigned keep;
  asm volatile("s_mov_b32 %0, m0\n\ts_mov_b32 m0, %2\n\ts_nop 0\n\tglobal_load_lds_dwordx4 %1, off sc1\n\ts_mov_b32 m0, %0"
               : "=&s"(keep)
               : "v"(g), "s"(ldsaddr)
               : "memory");
}
DI unsigned lds_addr(const void* p) { return (unsigned)(size_t)p; }

#define WAITV(n) asm volatile("s_waitcnt vmcnt(" #n ")" ::: "memory")
#define BARRIER() do { asm volatile("s_waitcnt lgkmcnt(0)" ::: "memory"); __builtin_amdgcn_s_barrier(); asm volatile("" ::: "memory"); } while (0)

DI int fresh_lane() {
  int l;
  asm volatile("v_mbcnt_lo_u32_b32 %0, -1, 0\n\tv_mbcnt_hi_u32_b32 %0, -1, %0" : "=v"(l));
  return l;
}
DI float max_halves(float m) {
  auto r = __builtin_amdgcn_permlane32_swap(__float_as_uint(m), __float_as_uint(m), false, false);
  return fmaxf(__uint_as_float(r[0]), __uint_as_float(r[1]));
}
DI float upper_half(float v) {
  auto r = __builtin_amdgcn_permlane32_swap(__float_as_uint(v), __float_as_uint(v), false, false);
  return __uint_as_float(r[1]);
}
DI bf16x8 tr_pair(const char* p0, const char* p1) {
  bf16x4 lo = __builtin_amdgcn_ds_read_tr16_b64_v4i16((lds_b4p)(p0));
  bf16x4 hi = __builtin_amdgcn_ds_read_tr16_b64_v4i16((lds_b4p)(p1));
  return __builtin_shufflevector(lo, hi, 0, 1, 2, 3, 4, 5, 6, 7);
}

constexpr int GSTG = 18432;
template <int NCT> constexpr int gboff() { return NCT == 5 ? 10240 : 8192; }
template <int NCT> constexpr int gstg() { return gboff<NCT>() + 8192; }
DI int colswz(int r) { return (((r >> 1) & 1) | (((r >> 3) & 1) << 1)) << 1; }

struct GemmSeg {
  const u16* src[5];
  size_t astep, bstep;
  int nk, kcur;
};
template <bool BROW>
DI void seg_init(GemmSeg& sg, const u16* A, int lda, const u16* B, int ldb, int nk, int k0, int tid) {
  sg.astep = (size_t)64 * lda;
  sg.bstep = BROW ? (size_t)64 : (size_t)64 * ldb;
  sg.nk = nk;
  sg.kcur = k0;
  A += (size_t)k0 * sg.astep;
  B += (size_t)k0 * sg.bstep;
#pragma unroll
  for (int i = 0; i < 2; ++i) {
    const int c = tid + 256 * i, r = c >> 3, ch = (c & 7) ^ colswz(r);
    sg.src[i] = A + (size_t)r * lda + ch * 8;
    if (BROW) sg.src[2 + i] = B + (size_t)r * ldb + ((c & 7) ^ ((r >> 1) & 7)) * 8;
    else sg.src[2 + i] = B + (size_t)r * ldb + ch * 8;
  }
  const int pos = (tid & 127) >> 1, r = pos ^ (((pos >> 3) & 1) << 2);
  sg.src[4] = A + (size_t)r * lda + 64 + (tid & 1) * 8;
}
template <int NCT, int DEV>
DI void seg_stage(GemmSeg& sg, unsigned b  , int wave) {
  if (DEV == 1) { glds16_dev(sg.src[0], b); glds16_dev(sg.src[1], b + 4096); }
  else { glds16(sg.src[0], b); glds16(sg.src[1], b + 4096); }
  if (DEV == 2) { glds16_dev(sg.src[2], b + gboff<NCT>()); glds16_dev(sg.src[3], b + gboff<NCT>() + 4096); }
  else { glds16(sg.src[2], b + gboff<NCT>()); glds16(sg.src[3], b + gboff<NCT>() + 4096); }
  if (NCT == 5 && wave < 2) {
    if (DEV == 1) glds16_dev(sg.src[4], b + 8192);
    else glds16(sg.src[4], b + 8192);
  }
  sg.kcur += 1;
  if (sg.kcur == sg.nk) {
    sg.kcur = 0;
    const size_t ab = sg.astep * (sg.nk - 1), bb = sg.bstep * (sg.nk - 1);
    sg.src[0] -= ab; sg.src[1] -= ab; sg.src[4] -= ab;
    sg.src[2] -= bb; sg.src[3] -= bb;
  } else {
    sg.src[0] += sg.astep; sg.src[1] += sg.astep; sg.src[4] += sg.astep;
    sg.src[2] += sg.bstep; sg.src[3] += sg.bstep;
  }
}
struct FragAddr { int aoff, asw, a2off, boffc, boffr, bsw, g; };
DI FragAddr frag_addr(int wave, int lane) {
  FragAddr f;
  const int g = lane >> 4, q = (lane & 15) >> 2, p = lane & 3;
  const int kr = 8 * g + q;
  f.g = g;
  f.aoff = kr * 128 + 8 * p;
  f.asw = colswz(kr);
  f.a2off = 8192 + (kr ^ (((kr >> 3) & 1) << 2)) * 32 + 8 * p;
  const int row = 16 * wave + (lane & 15);
  f.boffr = row * 128;
  f.boffc = kr * 128 + (((2 * wave) ^ f.asw) << 4) + 8 * p;
  f.bsw = (row >> 1) & 7;
  return f;
}
template <bool BROW, int NCT>
DI void gemm_step(f32x4 (&acc)[5], const char* base, const FragAddr& f) {
#pragma unroll
  for (int ks = 0; ks < 2; ++ks) {
    bf16x8 fa[5], fb;
#pragma unroll
    for (int ct = 0; ct < 4; ++ct) {
      const char* a = base + ks * 4096 + f.aoff + (((2 * ct) ^ f.asw) << 4);
      fa[ct] = tr_pair(a, a + 512);
    }
    if (NCT == 5) fa[4] = tr_pair(base + ks * 1024 + f.a2off, base + ks * 1024 + (f.a2off ^ 128));
    const char* bb = base + gboff<NCT>();
    if (BROW) fb = *(const bf16x8*)(bb + f.boffr + (((4 * ks + f.g) ^ f.bsw) << 4));
    else fb = tr_pair(bb + ks * 4096 + f.boffc, bb + ks * 4096 + f.boffc + 512);
#pragma unroll
    for (int ct = 0; ct < NCT; ++ct) acc[ct] = MFMA16(fa[ct], fb, acc[ct]);
  }
}
template <int NCT, int N>
DI void wait_stages(int wave) {
  if (NCT == 5 && wave < 2) asm volatile("s_waitcnt vmcnt(%0)" ::"i"(5 * N) : "memory");
  else asm volatile("s_waitcnt vmcnt(%0)" ::"i"(4 * N) : "memory");
}
template <int NCT, int N>
DI void loader_tail(int lw) {
  wait_stages<NCT, N>(lw);
  __builtin_amdgcn_s_barrier();
  if constexpr (N > 0) loader_tail<NCT, N - 1>(lw);
}
typedef __attribute__((address_space(1))) unsigned gu32;
typedef __attribute__((address_space(1))) unsigned long long gu64;
DI void wait_count(const char* cnt, unsigned target) {
  gu32* p = (gu32*)cnt;
#pragma unroll 1
  for (unsigned spins = 0; spins < (1u << 22); ++spins) {
    if ((unsigned)__builtin_amdgcn_readfirstlane(__hip_atomic_load(p, __ATOMIC_RELAXED, __HIP_MEMORY_SCOPE_AGENT)) >= target)
      break;
    __builtin_amdgcn_s_sleep(8);
  }
  __builtin_amdgcn_fence(__ATOMIC_ACQUIRE, "wavefront");
}
DI void store8_dev(void* p, u32x2 v) {
  __hip_atomic_store((gu64*)p, ((unsigned long long)v[1] << 32) | v[0], __ATOMIC_RELAXED, __HIP_MEMORY_SCOPE_AGENT);
}
template <int NCT, int NST, int DEV0, int DEV1>
DI void gemm_loader(GemmSeg& s0, int n0, GemmSeg& s1, int n1, char* lds, int lw, const char* cnt1 = nullptr,
                    unsigned tgt1 = 0) {
  const unsigned lbase = lds_addr(lds) + lw * 1024;
#pragma unroll 1
  for (int i = 0; i < NST - 1; ++i) seg_stage<NCT, DEV0>(s0, lbase + i * gstg<NCT>(), lw);
  int nb = NST - 1;
#pragma unroll 1
  for (int nx = NST - 1; nx < n0; ++nx) {
    wait_stages<NCT, NST - 2>(lw);
    __builtin_amdgcn_s_barrier();
    seg_stage<NCT, DEV0>(s0, lbase + nb * gstg<NCT>(), lw);
    nb = nb == NST - 1 ? 0 : nb + 1;
  }
  if (cnt1 != nullptr) {
    loader_tail<NCT, NST - 2>(lw);
    wait_count(cnt1, tgt1);
#pragma unroll 1
    for (int i = 0; i < NST - 2; ++i) {
      seg_stage<NCT, DEV1>(s1, lbase + nb * gstg<NCT>(), lw);
      nb = nb == NST - 1 ? 0 : nb + 1;
    }
#pragma unroll 1
    for (int nx = NST - 2; nx < n1; ++nx) {
      wait_stages<NCT, NST - 3>(lw);
      __builtin_amdgcn_s_barrier();
      seg_stage<NCT, DEV1>(s1, lbase + nb * gstg<NCT>(), lw);
      nb = nb == NST - 1 ? 0 : nb + 1;
    }
    loader_tail<NCT, NST - 3>(lw);
    return;
  }
#pragma unroll 1
  for (int nx = 0; nx < n1; ++nx) {
    wait_stages<NCT, NST - 2>(lw);
    __builtin_amdgcn_s_barrier();
    seg_stage<NCT, DEV1>(s1, lbase + nb * gstg<NCT>(), lw);
    nb = nb == NST - 1 ? 0 : nb + 1;
  }
  loader_tail<NCT, NST - 2>(lw);
}
template <bool BROW, int NCT, int NST>
DI void gemm_consume(f32x4 (&acc)[5], int nk, int& buf, char* lds, const FragAddr& f) {
#pragma unroll 1
  for (int kt = 0; kt < nk; ++kt) {
    BARRIER();
    gemm_step<BROW, NCT>(acc, lds + buf * gstg<NCT>(), f);
    buf = buf == NST - 1 ? 0 : buf + 1;
  }
}

constexpr int S1_NST = 4;
constexpr int S1_QT = 256;
constexpr int S1_KVT = 2 * (SEQ / 32);
constexpr int S1_LDS = S1_NST * GSTG;

__global__ __launch_bounds__(512) void stage1_kernel(const u16* __restrict__ x, const u16* __restrict__ wq,
                                                     const u16* __restrict__ wk, const u16* __restrict__ wv,
                                                     const u16* __restrict__ gw, char* __restrict__ ws,
                                                     const u16* __restrict__ w1, const u16* __restrict__ w2,
                                                     const u16* __restrict__ wo) {
  extern __shared__ __attribute__((aligned(16))) char lds[];
  const int tid = threadIdx.x, lane = tid & 63;
  const int wave = __builtin_amdgcn_readfirstlane(tid >> 6);
  int bid = blockIdx.x;
  if (bid >= S1_KVT + S1_QT) {
    const int i = bid - (S1_KVT + S1_QT);
    const u32x4* p1 = (const u32x4*)w1 + (size_t)i * 3072 + tid;
    const u32x4* p2 = (const u32x4*)w2 + (size_t)i * 3072 + tid;
    const u32x4* p3 = (const u32x4*)wo + (size_t)i * 400 + tid;
    u32x4 a[13];
#pragma unroll
    for (int k = 0; k < 6; ++k) { a[k] = p1[k * 512]; a[6 + k] = p2[k * 512]; }
    a[12] = tid < 400 ? p3[0] : a[0];
#pragma unroll
    for (int k = 0; k < 13; ++k) asm volatile("" ::"v"(a[k]));
    return;
  }
  if (bid == 0 && tid < CNT_N) *(unsigned*)(ws + WS_CNT + tid * 128) = 0u;
  if (bid >= S1_KVT) {
    bid -= S1_KVT;
    const int xcd = bid & 7, j = bid >> 3;
    const int ts = xcd * 4 + (j >> 3), tn = j & 7;
    const int n0 = tn * 80, s0 = ts * 64;
    if (wave >= 4) {
      GemmSeg sg;
      seg_init<true>(sg, wq + n0, DM, x + (size_t)s0 * DM, DM, DM / 64, (tn + 3 * (j >> 3)) % 10, tid - 256);
      gemm_loader<5, S1_NST, 0, 0>(sg, DM / 64, sg, 0, lds, wave - 4);
      return;
    }
    f32x4 acc[5] = {};
    {
      const FragAddr f = frag_addr(wave, lane);
      int buf = 0;
      gemm_consume<true, 5, S1_NST>(acc, DM / 64, buf, lds, f);
    }
    const float qs = 1.4426950408889634f * 0.22360679774997896f;
    u16* q = (u16*)(ws + WS_Q);
    const int s = s0 + 16 * wave + (lane & 15);
#pragma unroll
    for (int ct = 0; ct < 5; ++ct) {
      u32x2 v = {pk2(acc[ct][0] * qs, acc[ct][1] * qs), pk2(acc[ct][2] * qs, acc[ct][3] * qs)};
      *(u32x2*)(q + (size_t)s * DM + n0 + 16 * ct + 4 * (lane >> 4)) = v;
    }
    return;
  }
  {
    const int isv = bid & 1, s0 = (bid >> 1) * 32;
    char* ximg = lds;
    char* wimg = lds + 40960;
    {
      const u16* w = isv ? wv : wk;
      for (int i = wave; i < 65; i += 8) {
        if (i < 40) {
          const int kt = i >> 2, row = (i & 3) * 8 + (lane >> 3);
          const int c = (lane & 7) ^ ((row >> 1) & 7);
          glds16(x + (size_t)(s0 + row) * DM + kt * 64 + c * 8, lds_addr(ximg) + i * 1024);
        } else {
          glds16((const char*)w + (i - 40) * 1024 + lane * 16, lds_addr(wimg) + (i - 40) * 1024);
        }
      }
    }
    WAITV(0);
    BARRIER();
    const int hh = lane >> 5;
    if (wave < 4) {
      const int i16 = lane & 15, q4 = i16 >> 2, p4 = i16 & 3, g1 = (lane >> 4) & 1;
      const int row = lane & 31;
      f32x16 acc = {};
#pragma unroll
      for (int i = 0; i < 10; ++i) {
        const int k16 = wave * 10 + i;
        const int krow = 16 * k16 + 8 * hh + q4;
        const char* ap = wimg + krow * 40 + (16 * g1 + 4 * p4) * 2;
        bf16x8 fa = tr_pair(ap, ap + 4 * 40);
        const int c = 2 * (k16 & 3) + hh;
        bf16x8 fb = *(const bf16x8*)(ximg + (k16 >> 2) * 4096 + row * 128 + ((c ^ ((row >> 1) & 7)) << 4));
        acc = MFMA32(fa, fb, acc);
      }
      float* red = (float*)(lds + 40960 + 26624);
      (void)red;
      BARRIER();
      float* rb = (float*)lds;
      if (wave > 0) {
#pragma unroll
        for (int i = 0; i < 12; ++i) rb[((wave - 1) * 12 + i) * 64 + lane] = acc[i];
      }
      BARRIER();
      if (wave == 0) {
#pragma unroll
        for (int i = 0; i < 12; ++i) acc[i] += rb[i * 64 + lane] + rb[(12 + i) * 64 + lane] + rb[(24 + i) * 64 + lane];
        const int s = s0 + row;
        char* kvp = ws + WS_KV + (size_t)(s / KVCH) * KV_CHBYTES;
        const int key = s % KVCH;
        const int kb = key >> 4, r = key & 15, vh = (r >> 2) & 1, vj = (r & 3) + 4 * (r >> 3);
        char* vbase = kvp + KV_KBYTES + kb * KV_VSTR + vh * 352 + vj * 2;
#pragma unroll
        for (int g = 0; g < 3; ++g) {
          const int c0 = 8 * g + 4 * hh;
          if (c0 < 20) {
            if (!isv) {
              u32x2 v = {pk2(acc[4 * g], acc[4 * g + 1]), pk2(acc[4 * g + 2], acc[4 * g + 3])};
              *(u32x2*)(kvp + key * 48 + c0 * 2) = v;
            } else {
#pragma unroll
              for (int j = 0; j < 4; ++j) *(u16*)(vbase + (c0 + j) * 16) = f2bf(acc[4 * g + j]);
            }
          }
        }
        if (hh == 1) {
          if (!isv) {
            u32x2 v = {0x00003f80u, 0u};
            *(u32x2*)(kvp + key * 48 + 40) = v;
          } else {
            *(u16*)(vbase + 20 * 16) = 0x3f80;
            *(u16*)(vbase + 21 * 16) = 0;
          }
        }
      }
    } else {
      u16* h = (u16*)(ws + WS_H);
      float* gate = (float*)(ws + WS_G);
      const bool two = lane < 16;
      const u32x4 g0 = *(const u32x4*)(gw + lane * 8);
      u32x4 g1 = {0u, 0u, 0u, 0u};
      if (two) g1 = *(const u32x4*)(gw + 512 + lane * 8);
      u32x4 xa[4], xb[4];
#pragma unroll
      for (int rr = 0; rr < 4; ++rr) {
        const int row = isv * 16 + (wave - 4) * 4 + rr;
        const int sw = (row >> 1) & 7;
        xa[rr] = *(const u32x4*)(ximg + (lane >> 3) * 4096 + row * 128 + (((lane & 7) ^ sw) << 4));
        xb[rr] = u32x4{0u, 0u, 0u, 0u};
        if (two) xb[rr] = *(const u32x4*)(ximg + (8 + (lane >> 3)) * 4096 + row * 128 + (((lane & 7) ^ sw) << 4));
      }
      BARRIER();
      BARRIER();
      float sum[4], dot[4], var[4];
#pragma unroll
      for (int rr = 0; rr < 4; ++rr) {
        float sm = 0.f, dt = 0.f;
#pragma unroll
        for (int i = 0; i < 4; ++i) {
          const float a0 = __uint_as_float(xa[rr][i] << 16), a1 = __uint_as_float(xa[rr][i] & 0xffff0000u);
          const float b0 = __uint_as_float(xb[rr][i] << 16), b1 = __uint_as_float(xb[rr][i] & 0xffff0000u);
          sm += (a0 + a1) + (b0 + b1);
          dt += a0 * __uint_as_float(g0[i] << 16) + a1 * __uint_as_float(g0[i] & 0xffff0000u);
          dt += b0 * __uint_as_float(g1[i] << 16) + b1 * __uint_as_float(g1[i] & 0xffff0000u);
        }
        sum[rr] = sm; dot[rr] = dt;
      }
#pragma unroll
      for (int o = 32; o >= 1; o >>= 1) {
#pragma unroll
        for (int rr = 0; rr < 4; ++rr) { sum[rr] += __shfl_xor(sum[rr], o); dot[rr] += __shfl_xor(dot[rr], o); }
      }
#pragma unroll
      for (int rr = 0; rr < 4; ++rr) {
        const float mean = sum[rr] * (1.f / DM);
        sum[rr] = mean;
        float vr = 0.f;
#pragma unroll
        for (int i = 0; i < 4; ++i) {
          const float a0 = __uint_as_float(xa[rr][i] << 16) - mean, a1 = __uint_as_float(xa[rr][i] & 0xffff0000u) - mean;
          const float b0 = __uint_as_float(xb[rr][i] << 16) - mean, b1 = __uint_as_float(xb[rr][i] & 0xffff0000u) - mean;
          vr += a0 * a0 + a1 * a1;
          if (two) vr += b0 * b0 + b1 * b1;
        }
        var[rr] = vr;
      }
#pragma unroll
      for (int o = 32; o >= 1; o >>= 1) {
#pragma unroll
        for (int rr = 0; rr < 4; ++rr) var[rr] += __shfl_xor(var[rr], o);
      }
#pragma unroll
      for (int rr = 0; rr < 4; ++rr) {
        const int s = s0 + isv * 16 + (wave - 4) * 4 + rr;
        const float mean = sum[rr];
        const float rstd = 1.f / sqrtf(var[rr] * (1.f / DM) + 1e-5f);
        u32x4 o0, o1;
#pragma unroll
        for (int i = 0; i < 4; ++i) {
          o0[i] = pk2((__uint_as_float(xa[rr][i] << 16) - mean) * rstd, (__uint_as_float(xa[rr][i] & 0xffff0000u) - mean) * rstd);
          o1[i] = pk2((__uint_as_float(xb[rr][i] << 16) - mean) * rstd, (__uint_as_float(xb[rr][i] & 0xffff0000u) - mean) * rstd);
        }
        *(u32x4*)(h + (size_t)s * DM + lane * 8) = o0;
        if (two) *(u32x4*)(h + (size_t)s * DM + 512 + lane * 8) = o1;
        if (lane == 0) gate[s] = 1.f / (1.f + __expf(-dot[rr]));
      }
    }
  }
}

constexpr int G1_TILES = 240;
constexpr int GM_WGS = 256;
constexpr int AT_WGS = SEQ / 8;
constexpr int G_NST = 4;
constexpr int AT_STG = 2 * KV_CHBYTES;
constexpr int S2_FLAG = 3 * AT_STG;
constexpr int S2_SHIFT = S2_FLAG + 64;
constexpr int S2_LDS = S2_SHIFT + 4096;

template <bool SAFE>
DI void attn_pass(const char* __restrict__ ws, char* lds, int srow, f32x16& o, int wave, int lane) {
  const int hd = lane & 31, hh = lane >> 5;
  const int half = wave >> 3;
  const char* kvg = ws + WS_KV;
  bf16x8 qf0;
  bf16x4 qf1;
  {
    const u16* q = (const u16*)(ws + WS_Q) + (size_t)srow * DM + hd * DH;
    u32x2 a = *(const u32x2*)(q + 8 * hh), b = *(const u32x2*)(q + 8 * hh + 4), c = *(const u32x2*)(q + 16);
    u32x4 t0 = {a[0], a[1], b[0], b[1]};
    u32x2 t1 = {hh == 0 ? c[0] : 0u, hh == 0 ? c[1] : 0u};
    qf0 = __builtin_bit_cast(bf16x8, t0);
    qf1 = __builtin_bit_cast(bf16x4, t1);
  }
  const int koff0 = half * KV_CHBYTES + hd * 48 + 16 * hh, koff1 = half * KV_CHBYTES + hd * 48 + 32 + 8 * hh;
  const int voff = half * KV_CHBYTES + KV_KBYTES + hh * 352 + (hd < 21 ? hd : 21) * 16;
  const int pc0 = wave, pc1 = 16 + wave;
  const char* gsrc0 = kvg + (size_t)((pc0 / 12) * (NCH / 2)) * KV_CHBYTES + (pc0 % 12) * 1024 + lane * 16;
  const int gd1 = (NCH / 2) * KV_CHBYTES + (pc1 - 12) * 1024 - pc0 * 1024;
  const unsigned ldst = lds_addr(lds);
  auto stage = [&](int boff) {
    glds16(gsrc0, ldst + boff + pc0 * 1024);
    if (wave < 8) glds16(gsrc0 + gd1, ldst + boff + pc1 * 1024);
    gsrc0 += KV_CHBYTES;
  };
  float mrun = -3.0e38f;
  stage(0);
  stage(AT_STG);
  if (wave < 8) WAITV(2); else WAITV(1);
  BARRIER();
  int bcur = 0;
  bf16x8 k0 = *(const bf16x8*)(lds + koff0);
  bf16x4 k1 = *(const bf16x4*)(lds + koff1);
  bf16x8 v0 = *(const bf16x8*)(lds + voff);
  bf16x8 v1 = *(const bf16x8*)(lds + voff + KV_VSTR);
  if (!SAFE) {
    f32x16 z = {};
    f32x16 s = MFMA32(k0, qf0, z);
    s = MFMA32K8(k1, qf1, s);
    float m = s[0];
#pragma unroll
    for (int i = 1; i < 16; ++i) m = fmaxf(m, s[i]);
    m = max_halves(m);
    const u16 mb = f2bf(m);
    *(float*)(lds + S2_SHIFT + (wave * 64 + lane) * 4) = bf2f(mb);
    if (hh == 1) qf1[0] = (short)(mb ^ 0x8000u);
  }
#pragma unroll 1
  for (int ch = 0; ch < NCH / 2; ++ch) {
    const char* base = lds + bcur;
    const int bnext = bcur == 2 * AT_STG ? 0 : bcur + AT_STG;
#pragma unroll SAFE ? 1 : 4
    for (int t = 0; t < KVCH / 32; ++t) {
      f32x16 z = {};
      f32x16 s = MFMA32(k0, qf0, z);
      s = MFMA32K8(k1, qf1, s);
      bf16x8 cv0 = v0, cv1 = v1;
      if (t + 1 < KVCH / 32) {
        k0 = *(const bf16x8*)(base + (t + 1) * 32 * 48 + koff0);
        k1 = *(const bf16x4*)(base + (t + 1) * 32 * 48 + koff1);
        v0 = *(const bf16x8*)(base + voff + (2 * t + 2) * KV_VSTR);
        v1 = *(const bf16x8*)(base + voff + (2 * t + 3) * KV_VSTR);
      } else {
        WAITV(0);
        BARRIER();
        stage(bnext == 2 * AT_STG ? 0 : bnext + AT_STG);
        const char* nb = lds + bnext;
        k0 = *(const bf16x8*)(nb + koff0);
        k1 = *(const bf16x4*)(nb + koff1);
        v0 = *(const bf16x8*)(nb + voff);
        v1 = *(const bf16x8*)(nb + voff + KV_VSTR);
      }
      if (SAFE) {
        float m = s[0];
#pragma unroll
        for (int i = 1; i < 16; ++i) m = fmaxf(m, s[i]);
        m = max_halves(m);
        const float mn = fmaxf(mrun, m);
        const float alpha = __builtin_amdgcn_exp2f(mrun - mn);
        mrun = mn;
#pragma unroll
        for (int i = 0; i < 16; ++i) { o[i] *= alpha; s[i] -= mn; }
      }
      u32x4 p0, p1;
#pragma unroll
      for (int i = 0; i < 4; ++i) {
        p0[i] = pk2(__builtin_amdgcn_exp2f(s[2 * i]), __builtin_amdgcn_exp2f(s[2 * i + 1]));
        p1[i] = pk2(__builtin_amdgcn_exp2f(s[8 + 2 * i]), __builtin_amdgcn_exp2f(s[8 + 2 * i + 1]));
      }
      o = MFMA32(cv0, __builtin_bit_cast(bf16x8, p0), o);
      o = MFMA32(cv1, __builtin_bit_cast(bf16x8, p1), o);
    }
    bcur = bnext;
  }
  WAITV(0);
  if (SAFE) *(float*)(lds + S2_SHIFT + (wave * 64 + lane) * 4) = mrun;
}

__global__ __launch_bounds__(1024, 6) void stage2_kernel(const u16* __restrict__ w1, const u16* __restrict__ b1,
                                                         const u16* __restrict__ wo, const u16* __restrict__ w2,
                                                         const u16* __restrict__ b2, char* __restrict__ ws,
                                                         u16* __restrict__ out) {
  extern __shared__ __attribute__((aligned(16))) char lds[];
  const int tid = threadIdx.x, lane = tid & 63;
  const int wave = __builtin_amdgcn_readfirstlane(tid >> 6);
  if (blockIdx.x < GM_WGS) {
    if (wave >= 8) return;
    const int bid = blockIdx.x;
    const int xcd = bid & 7, j = bid >> 3;
    const char* cnt = ws + WS_CNT;
    u16* u = (u16*)(ws + WS_U);
    const int ts = xcd * 4 + (j >> 3), td = j & 7;
    const int d0 = td * 80, s0 = ts * 64;
    if (wave >= 4) {
      if (j < 30) {
        const int tf = xcd * 3 + j / 10, t1 = j % 10;
        GemmSeg sg;
        seg_init<false>(sg, (const u16*)(ws + WS_H) + t1 * 64, DM, w1 + tf * 64, FF, SEQ / 64, (t1 * 3 + (j / 10)) & 31,
                        tid - 256);
        gemm_loader<4, G_NST, 0, 0>(sg, SEQ / 64, sg, 0, lds, wave - 4);
        __builtin_amdgcn_s_barrier();
      }
      wait_count(cnt + (CNT_U + d0 / 64) * 128, 24);
      wait_count(cnt + (CNT_U + (d0 + 79) / 64) * 128, 24);
      GemmSeg sb, sa;
      seg_init<false>(sb, u + d0, DM, w2 + s0, SEQ, FF / 64, td * 3, tid - 256);
      seg_init<true>(sa, wo + d0, DM, (const u16*)(ws + WS_O) + (size_t)s0 * DM, DM, DM / 64, (td + 3 * (j >> 3)) % 10,
                     tid - 256);
      gemm_loader<5, G_NST, 1, 2>(sb, FF / 64, sa, DM / 64, lds, wave - 4, cnt + (CNT_O + ts) * 128, 8);
      return;
    }
    const FragAddr f = frag_addr(wave, lane);
    if (j < 30) {
      const int tf = xcd * 3 + j / 10, t1 = j % 10;
      const int fcol = tf * 64 + 16 * wave + (lane & 15);
      const float bias = bf2f(b1[fcol]);
      f32x4 acc[5] = {};
      int buf = 0;
      gemm_consume<false, 4, G_NST>(acc, SEQ / 64, buf, lds, f);
#pragma unroll
      for (int ct = 0; ct < 4; ++ct) {
        float r[4];
#pragma unroll
        for (int i = 0; i < 4; ++i) {
          const float t = acc[ct][i] + bias;
          const float z = 0.7978845608028654f * (t + 0.044715f * t * t * t);
          r[i] = t / (1.f + __expf(-2.f * z));
        }
        u32x2 v = {pk2(r[0], r[1]), pk2(r[2], r[3])};
        store8_dev(u + (size_t)fcol * DM + t1 * 64 + 16 * ct + 4 * (lane >> 4), v);
      }
      WAITV(0);
      BARRIER();
      if (tid == 0)
        __hip_atomic_fetch_add((gu32*)(cnt + (CNT_U + t1) * 128), 1u, __ATOMIC_RELAXED, __HIP_MEMORY_SCOPE_AGENT);
    }
    const int s = s0 + 16 * wave + (lane & 15);
    const float g = ((const float*)(ws + WS_G))[s], bias = bf2f(b2[s]);
    f32x4 acc[5] = {};
    {
      int buf = 0;
      gemm_consume<false, 5, G_NST>(acc, FF / 64, buf, lds, f);
#pragma unroll
      for (int ct = 0; ct < 5; ++ct)
#pragma unroll
        for (int i = 0; i < 4; ++i) acc[ct][i] = (acc[ct][i] + bias) * (1.f - g);
      gemm_consume<true, 5, G_NST>(acc, DM / 64, buf, lds, f);
    }
#pragma unroll
    for (int ct = 0; ct < 5; ++ct) {
      u32x2 v = {pk2(acc[ct][0], acc[ct][1]), pk2(acc[ct][2], acc[ct][3])};
      *(u32x2*)(out + (size_t)s * DM + d0 + 16 * ct + 4 * (lane >> 4)) = v;
    }
    return;
  }
  const int half = wave >> 3;
  const int s = (blockIdx.x - GM_WGS) * 8 + (wave & 7);
  f32x16 o = {};
  volatile unsigned* flag = (volatile unsigned*)(lds + S2_FLAG);
  if (tid == 0) *flag = 0u;
  attn_pass<false>(ws, lds, s, o, wave, lane);

  {
    const float l = upper_half(o[8]);
    const bool bad = !(l > 8.67e-19f && l < 1.15e18f);
    if (bad) *flag = 1u;
    __syncthreads();
    if (__builtin_amdgcn_readfirstlane(*flag) != 0u) {
      o = f32x16{};
      attn_pass<true>(ws, lds, s, o, wave, fresh_lane());
    }
  }
  const int lane2 = fresh_lane();
  const int hd = lane2 & 31, hh = lane2 >> 5;
  __syncthreads();
  float* mg = (float*)lds + (wave & 7) * 13 * 64;
  if (half == 1) {
#pragma unroll
    for (int i = 0; i < 12; ++i) mg[i * 64 + lane2] = o[i];
  }
  __syncthreads();
  if (half == 0) {
    const float shift = *(const float*)(lds + S2_SHIFT + (wave * 64 + lane2) * 4);
    const float sh1 = *(const float*)(lds + S2_SHIFT + ((wave + 8) * 64 + lane2) * 4);
    const float mm = fmaxf(shift, sh1);
    const float f0 = __builtin_amdgcn_exp2f(shift - mm), f1 = __builtin_amdgcn_exp2f(sh1 - mm);
#pragma unroll
    for (int i = 0; i < 12; ++i) o[i] = o[i] * f0 + mg[i * 64 + lane2] * f1;
    const float l = upper_half(o[8]);
    const float inv = ((const float*)(ws + WS_G))[s] / l;
    u16* op = (u16*)(ws + WS_O) + (size_t)s * DM + hd * DH + 4 * hh;
#pragma unroll
    for (int g = 0; g < 3; ++g) {
      if (g < 2 || hh == 0) {
        u32x2 v = {pk2(o[4 * g] * inv, o[4 * g + 1] * inv), pk2(o[4 * g + 2] * inv, o[4 * g + 3] * inv)};
        store8_dev(op + 8 * g, v);
      }
    }
    WAITV(0);
  }
  __syncthreads();
  if (wave == 0 && lane2 == 0)
    __hip_atomic_fetch_add((gu32*)(ws + WS_CNT + (CNT_O + (s >> 6)) * 128), 1u, __ATOMIC_RELAXED,
                           __HIP_MEMORY_SCOPE_AGENT);
}

extern "C" void kernel_launch(void* const* d_in, const int* in_sizes, int n_in, void* d_out, int out_size, void* d_ws,
                              size_t ws_size, hipStream_t stream) {
  const u16* x = (const u16*)d_in[0];
  const u16* wq = (const u16*)d_in[1];
  const u16* wk = (const u16*)d_in[2];
  const u16* wv = (const u16*)d_in[3];
  const u16* wo = (const u16*)d_in[4];
  const u16* w1 = (const u16*)d_in[6];
  const u16* b1 = (const u16*)d_in[7];
  const u16* w2 = (const u16*)d_in[8];
  const u16* b2 = (const u16*)d_in[9];
  const u16* gw = (const u16*)d_in[10];
  char* ws = (char*)d_ws;
  (void)hipFuncSetAttribute((const void*)stage1_kernel, hipFuncAttributeMaxDynamicSharedMemorySize, S1_LDS);
  (void)hipFuncSetAttribute((const void*)stage2_kernel, hipFuncAttributeMaxDynamicSharedMemorySize, S2_LDS);
  stage1_kernel<<<S1_QT + S1_KVT + 128, 512, S1_LDS, stream>>>(x, wq, wk, wv, gw, ws, w1, w2, wo);
  stage2_kernel<<<GM_WGS + AT_WGS, 1024, S2_LDS, stream>>>(w1, b1, wo, w2, b2, ws, (u16*)d_out);
}
```

```cpp
#include <hip/hip_runtime.h>
#include <hip/hip_bf16.h>
#include <stdint.h>

typedef short bf16x8 __attribute__((ext_vector_type(8)));
typedef short bf16x4 __attribute__((ext_vector_type(4)));
typedef float f32x16 __attribute__((ext_vector_type(16)));
typedef float f32x4 __attribute__((ext_vector_type(4)));
typedef float f32x2 __attribute__((ext_vector_type(2)));
typedef __bf16 bfx2 __attribute__((ext_vector_type(2)));
typedef unsigned u32x4 __attribute__((ext_vector_type(4)));
typedef unsigned u32x2 __attribute__((ext_vector_type(2)));
typedef unsigned short u16;
typedef __attribute__((address_space(3))) bf16x4* lds_b4p;

#define DI __device__ __forceinline__
#define MFMA32(a, b, c) __builtin_amdgcn_mfma_f32_32x32x16_bf16((a), (b), (c), 0, 0, 0)
#define MFMA32K8(a, b, c) __builtin_amdgcn_mfma_f32_32x32x8bf16_1k((a), (b), (c), 0, 0, 0)
#define MFMA16(a, b, c) __builtin_amdgcn_mfma_f32_16x16x32_bf16((a), (b), (c), 0, 0, 0)

constexpr int SEQ = 2048, DM = 640, NH = 32, DH = 20, FF = 1536;
constexpr int KVCH = 128;
constexpr int KV_KBYTES = KVCH * 48;
constexpr int KV_VSTR = 704;
constexpr int KV_CHBYTES = 12288;
constexpr int NCH = SEQ / KVCH;

constexpr size_t WS_Q = 0;
constexpr size_t WS_H = WS_Q + (size_t)SEQ * DM * 2;
constexpr size_t WS_O = WS_H + (size_t)SEQ * DM * 2;
constexpr size_t WS_U = WS_O + (size_t)SEQ * DM * 2;
constexpr size_t WS_KV = WS_U + (size_t)FF * DM * 2;
constexpr size_t WS_G = WS_KV + (size_t)NCH * KV_CHBYTES;
constexpr size_t WS_CNT = WS_G + (size_t)SEQ * 4;
constexpr int CNT_U = 0, CNT_O = 10, CNT_N = 42;

DI unsigned pk2(float lo, float hi) {
  f32x2 v = {lo, hi};
  bfx2 b = __builtin_convertvector(v, bfx2);
  return __builtin_bit_cast(unsigned, b);
}
DI float bf2f(u16 v) { return __uint_as_float(((unsigned)v) << 16); }
DI u16 f2bf(float f) { return (u16)(pk2(f, 0.f) & 0xffffu); }

DI void glds16(const void* g, unsigned ldsaddr) {
  unsigned keep;
  asm volatile("s_mov_b32 %0, m0\n\ts_mov_b32 m0, %2\n\ts_nop 0\n\tglobal_load_lds_dwordx4 %1, off\n\ts_mov_b32 m0, %0"
               : "=&s"(keep)
               : "v"(g), "s"(ldsaddr)
               : "memory");
}
DI void glds16_dev(const void* g, unsigned ldsaddr) {
  unsigned keep;
  asm volatile("s_mov_b32 %0, m0\n\ts_mov_b32 m0, %2\n\ts_nop 0\n\tglobal_load_lds_dwordx4 %1, off sc1\n\ts_mov_b32 m0, %0"
               : "=&s"(keep)
               : "v"(g), "s"(ldsaddr)
               : "memory");
}
DI unsigned lds_addr(const void* p) { return (unsigned)(size_t)p; }

#define WAITV(n) asm volatile("s_waitcnt vmcnt(" #n ")" ::: "memory")
#define BARRIER() do { asm volatile("s_waitcnt lgkmcnt(0)" ::: "memory"); __builtin_amdgcn_s_barrier(); asm volatile("" ::: "memory"); } while (0)

DI int fresh_lane() {
  int l;
  asm volatile("v_mbcnt_lo_u32_b32 %0, -1, 0\n\tv_mbcnt_hi_u32_b32 %0, -1, %0" : "=v"(l));
  return l;
}
DI float max_halves(float m) {
  auto r = __builtin_amdgcn_permlane32_swap(__float_as_uint(m), __float_as_uint(m), false, false);
  return fmaxf(__uint_as_float(r[0]), __uint_as_float(r[1]));
}
DI float upper_half(float v) {
  auto r = __builtin_amdgcn_permlane32_swap(__float_as_uint(v), __float_as_uint(v), false, false);
  return __uint_as_float(r[1]);
}
DI float wave_sum(float v) {
  v += __uint_as_float(__builtin_amdgcn_update_dpp(0u, __float_as_uint(v), 0xB1, 0xf, 0xf, false));
  v += __uint_as_float(__builtin_amdgcn_update_dpp(0u, __float_as_uint(v), 0x4E, 0xf, 0xf, false));
  v += __uint_as_float(__builtin_amdgcn_update_dpp(0u, __float_as_uint(v), 0x141, 0xf, 0xf, false));
  v += __uint_as_float(__builtin_amdgcn_update_dpp(0u, __float_as_uint(v), 0x140, 0xf, 0xf, false));
  auto a = __builtin_amdgcn_permlane16_swap(__float_as_uint(v), __float_as_uint(v), false, false);
  v = __uint_as_float(a[0]) + __uint_as_float(a[1]);
  auto b = __builtin_amdgcn_permlane32_swap(__float_as_uint(v), __float_as_uint(v), false, false);
  return __uint_as_float(b[0]) + __uint_as_float(b[1]);
}
DI bf16x8 tr_pair(const char* p0, const char* p1) {
  bf16x4 lo = __builtin_amdgcn_ds_read_tr16_b64_v4i16((lds_b4p)(p0));
  bf16x4 hi = __builtin_amdgcn_ds_read_tr16_b64_v4i16((lds_b4p)(p1));
  return __builtin_shufflevector(lo, hi, 0, 1, 2, 3, 4, 5, 6, 7);
}

constexpr int GSTG = 18432;
template <int NCT> constexpr int gboff() { return NCT == 5 ? 10240 : 8192; }
template <int NCT> constexpr int gstg() { return gboff<NCT>() + 8192; }
DI int colswz(int r) { return (((r >> 1) & 1) | (((r >> 3) & 1) << 1)) << 1; }

struct GemmSeg {
  const u16* src[5];
  size_t astep, bstep;
  int nk, kcur;
};
template <bool BROW>
DI void seg_init(GemmSeg& sg, const u16* A, int lda, const u16* B, int ldb, int nk, int k0, int tid) {
  sg.astep = (size_t)64 * lda;
  sg.bstep = BROW ? (size_t)64 : (size_t)64 * ldb;
  sg.nk = nk;
  sg.kcur = k0;
  A += (size_t)k0 * sg.astep;
  B += (size_t)k0 * sg.bstep;
#pragma unroll
  for (int i = 0; i < 2; ++i) {
    const int c = tid + 256 * i, r = c >> 3, ch = (c & 7) ^ colswz(r);
    sg.src[i] = A + (size_t)r * lda + ch * 8;
    if (BROW) sg.src[2 + i] = B + (size_t)r * ldb + ((c & 7) ^ ((r >> 1) & 7)) * 8;
    else sg.src[2 + i] = B + (size_t)r * ldb + ch * 8;
  }
  const int pos = (tid & 127) >> 1, r = pos ^ (((pos >> 3) & 1) << 2);
  sg.src[4] = A + (size_t)r * lda + 64 + (tid & 1) * 8;
}
template <int NCT, int DEV>
DI void seg_stage(GemmSeg& sg, unsigned b  , int wave) {
  if (DEV == 1) { glds16_dev(sg.src[0], b); glds16_dev(sg.src[1], b + 4096); }
  else { glds16(sg.src[0], b); glds16(sg.src[1], b + 4096); }
  if (DEV == 2) { glds16_dev(sg.src[2], b + gboff<NCT>()); glds16_dev(sg.src[3], b + gboff<NCT>() + 4096); }
  else { glds16(sg.src[2], b + gboff<NCT>()); glds16(sg.src[3], b + gboff<NCT>() + 4096); }
  if (NCT == 5 && wave < 2) {
    if (DEV == 1) glds16_dev(sg.src[4], b + 8192);
    else glds16(sg.src[4], b + 8192);
  }
  sg.kcur += 1;
  if (sg.kcur == sg.nk) {
    sg.kcur = 0;
    const size_t ab = sg.astep * (sg.nk - 1), bb = sg.bstep * (sg.nk - 1);
    sg.src[0] -= ab; sg.src[1] -= ab; sg.src[4] -= ab;
    sg.src[2] -= bb; sg.src[3] -= bb;
  } else {
    sg.src[0] += sg.astep; sg.src[1] += sg.astep; sg.src[4] += sg.astep;
    sg.src[2] += sg.bstep; sg.src[3] += sg.bstep;
  }
}
struct FragAddr { int aoff, asw, a2off, boffc, boffr, bsw, g; };
DI FragAddr frag_addr(int wave, int lane) {
  FragAddr f;
  const int g = lane >> 4, q = (lane & 15) >> 2, p = lane & 3;
  const int kr = 8 * g + q;
  f.g = g;
  f.aoff = kr * 128 + 8 * p;
  f.asw = colswz(kr);
  f.a2off = 8192 + (kr ^ (((kr >> 3) & 1) << 2)) * 32 + 8 * p;
  const int row = 16 * wave + (lane & 15);
  f.boffr = row * 128;
  f.boffc = kr * 128 + (((2 * wave) ^ f.asw) << 4) + 8 * p;
  f.bsw = (row >> 1) & 7;
  return f;
}
template <bool BROW, int NCT>
DI void gemm_step(f32x4 (&acc)[5], const char* base, const FragAddr& f) {
#pragma unroll
  for (int ks = 0; ks < 2; ++ks) {
    bf16x8 fa[5], fb;
#pragma unroll
    for (int ct = 0; ct < 4; ++ct) {
      const char* a = base + ks * 4096 + f.aoff + (((2 * ct) ^ f.asw) << 4);
      fa[ct] = tr_pair(a, a + 512);
    }
    if (NCT == 5) fa[4] = tr_pair(base + ks * 1024 + f.a2off, base + ks * 1024 + (f.a2off ^ 128));
    const char* bb = base + gboff<NCT>();
    if (BROW) fb = *(const bf16x8*)(bb + f.boffr + (((4 * ks + f.g) ^ f.bsw) << 4));
    else fb = tr_pair(bb + ks * 4096 + f.boffc, bb + ks * 4096 + f.boffc + 512);
#pragma unroll
    for (int ct = 0; ct < NCT; ++ct) acc[ct] = MFMA16(fa[ct], fb, acc[ct]);
  }
}
template <int NCT, int N>
DI void wait_stages(int wave) {
  if (NCT == 5 && wave < 2) asm volatile("s_waitcnt vmcnt(%0)" ::"i"(5 * N) : "memory");
  else asm volatile("s_waitcnt vmcnt(%0)" ::"i"(4 * N) : "memory");
}
template <int NCT, int N>
DI void loader_tail(int lw) {
  wait_stages<NCT, N>(lw);
  __builtin_amdgcn_s_barrier();
  if constexpr (N > 0) loader_tail<NCT, N - 1>(lw);
}
typedef __attribute__((address_space(1))) unsigned gu32;
typedef __attribute__((address_space(1))) unsigned long long gu64;
DI void wait_count(const char* cnt, unsigned target) {
  gu32* p = (gu32*)cnt;
#pragma unroll 1
  for (unsigned spins = 0; spins < (1u << 22); ++spins) {
    if ((unsigned)__builtin_amdgcn_readfirstlane(__hip_atomic_load(p, __ATOMIC_RELAXED, __HIP_MEMORY_SCOPE_AGENT)) >= target)
      break;
    __builtin_amdgcn_s_sleep(8);
  }
  __builtin_amdgcn_fence(__ATOMIC_ACQUIRE, "wavefront");
}
DI void store8_dev(void* p, u32x2 v) {
  __hip_atomic_store((gu64*)p, ((unsigned long long)v[1] << 32) | v[0], __ATOMIC_RELAXED, __HIP_MEMORY_SCOPE_AGENT);
}
template <int NCT, int NST, int DEV0, int DEV1>
DI void gemm_loader(GemmSeg& s0, int n0, GemmSeg& s1, int n1, char* lds, int lw, const char* cnt1 = nullptr,
                    unsigned tgt1 = 0) {
  const unsigned lbase = lds_addr(lds) + lw * 1024;
#pragma unroll 1
  for (int i = 0; i < NST - 1; ++i) seg_stage<NCT, DEV0>(s0, lbase + i * gstg<NCT>(), lw);
  int nb = NST - 1;
#pragma unroll 1
  for (int nx = NST - 1; nx < n0; ++nx) {
    wait_stages<NCT, NST - 2>(lw);
    __builtin_amdgcn_s_barrier();
    seg_stage<NCT, DEV0>(s0, lbase + nb * gstg<NCT>(), lw);
    nb = nb == NST - 1 ? 0 : nb + 1;
  }
  if (cnt1 != nullptr) {
    loader_tail<NCT, NST - 2>(lw);
    wait_count(cnt1, tgt1);
#pragma unroll 1
    for (int i = 0; i < NST - 2; ++i) {
      seg_stage<NCT, DEV1>(s1, lbase + nb * gstg<NCT>(), lw);
      nb = nb == NST - 1 ? 0 : nb + 1;
    }
#pragma unroll 1
    for (int nx = NST - 2; nx < n1; ++nx) {
      wait_stages<NCT, NST - 3>(lw);
      __builtin_amdgcn_s_barrier();
      seg_stage<NCT, DEV1>(s1, lbase + nb * gstg<NCT>(), lw);
      nb = nb == NST - 1 ? 0 : nb + 1;
    }
    loader_tail<NCT, NST - 3>(lw);
    return;
  }
#pragma unroll 1
  for (int nx = 0; nx < n1; ++nx) {
    wait_stages<NCT, NST - 2>(lw);
    __builtin_amdgcn_s_barrier();
    seg_stage<NCT, DEV1>(s1, lbase + nb * gstg<NCT>(), lw);
    nb = nb == NST - 1 ? 0 : nb + 1;
  }
  loader_tail<NCT, NST - 2>(lw);
}
template <bool BROW, int NCT, int NST>
DI void gemm_consume(f32x4 (&acc)[5], int nk, int& buf, char* lds, const FragAddr& f) {
#pragma unroll 1
  for (int kt = 0; kt < nk; ++kt) {
    BARRIER();
    gemm_step<BROW, NCT>(acc, lds + buf * gstg<NCT>(), f);
    buf = buf == NST - 1 ? 0 : buf + 1;
  }
}

constexpr int S1_NST = 4;
constexpr int S1_QT = 256;
constexpr int S1_KVT = 2 * (SEQ / 32);
constexpr int S1_LDS = S1_NST * GSTG;

__global__ __launch_bounds__(512) void stage1_kernel(const u16* __restrict__ x, const u16* __restrict__ wq,
                                                     const u16* __restrict__ wk, const u16* __restrict__ wv,
                                                     const u16* __restrict__ gw, char* __restrict__ ws) {
  extern __shared__ __attribute__((aligned(16))) char lds[];
  const int tid = threadIdx.x, lane = tid & 63;
  const int wave = __builtin_amdgcn_readfirstlane(tid >> 6);
  int bid = blockIdx.x;
  if (bid == 0 && tid < CNT_N) *(unsigned*)(ws + WS_CNT + tid * 128) = 0u;
  if (bid >= S1_KVT) {
    bid -= S1_KVT;
    const int xcd = bid & 7, j = bid >> 3;
    const int ts = xcd * 4 + (j >> 3), tn = j & 7;
    const int n0 = tn * 80, s0 = ts * 64;
    if (wave >= 4) {
      GemmSeg sg;
      seg_init<true>(sg, wq + n0, DM, x + (size_t)s0 * DM, DM, DM / 64, (tn + 3 * (j >> 3)) % 10, tid - 256);
      gemm_loader<5, S1_NST, 0, 0>(sg, DM / 64, sg, 0, lds, wave - 4);
      return;
    }
    f32x4 acc[5] = {};
    {
      const FragAddr f = frag_addr(wave, lane);
      int buf = 0;
      gemm_consume<true, 5, S1_NST>(acc, DM / 64, buf, lds, f);
    }
    const float qs = 1.4426950408889634f * 0.22360679774997896f;
    u16* q = (u16*)(ws + WS_Q);
    const int s = s0 + 16 * wave + (lane & 15);
#pragma unroll
    for (int ct = 0; ct < 5; ++ct) {
      u32x2 v = {pk2(acc[ct][0] * qs, acc[ct][1] * qs), pk2(acc[ct][2] * qs, acc[ct][3] * qs)};
      *(u32x2*)(q + (size_t)s * DM + n0 + 16 * ct + 4 * (lane >> 4)) = v;
    }
    return;
  }
  {
    const int isv = bid & 1, s0 = (bid >> 1) * 32;
    char* ximg = lds;
    char* wimg = lds + 40960;
    const bool two = lane < 16;
    const u32x4 g0 = *(const u32x4*)(gw + lane * 8);
    u32x4 g1 = {0u, 0u, 0u, 0u};
    if (two) g1 = *(const u32x4*)(gw + 512 + lane * 8);
    {
      const u16* w = isv ? wv : wk;
      for (int i = wave; i < 65; i += 8) {
        if (i < 40) {
          const int kt = i >> 2, row = (i & 3) * 8 + (lane >> 3);
          const int c = (lane & 7) ^ ((row >> 1) & 7);
          glds16(x + (size_t)(s0 + row) * DM + kt * 64 + c * 8, lds_addr(ximg) + i * 1024);
        } else {
          glds16((const char*)w + (i - 40) * 1024 + lane * 16, lds_addr(wimg) + (i - 40) * 1024);
        }
      }
    }
    WAITV(0);
    BARRIER();
    if (wave < 4) {
      const int wm = wave >> 1, wn = wave & 1;
      const int g = lane >> 4, q4 = (lane & 15) >> 2, p4 = lane & 3;
      const int row = 16 * wn + (lane & 15);
      const int rsw = (row >> 1) & 7;
      const char* ap = wimg + (8 * g + q4) * 40 + (16 * wm + 4 * p4) * 2;
      f32x4 acc = {};
#pragma unroll 4
      for (int i = 0; i < 20; ++i) {
        bf16x8 fa = tr_pair(ap + i * 32 * 40, ap + i * 32 * 40 + 4 * 40);
        bf16x8 fb = *(const bf16x8*)(ximg + (i >> 1) * 4096 + row * 128 + (((4 * (i & 1) + g) ^ rsw) << 4));
        acc = MFMA16(fa, fb, acc);
      }
      const int s = s0 + row;
      char* kvp = ws + WS_KV + (size_t)(s / KVCH) * KV_CHBYTES;
      const int key = s % KVCH;
      const int kb = key >> 4, r = key & 15, vh = (r >> 2) & 1, vj = (r & 3) + 4 * (r >> 3);
      char* vbase = kvp + KV_KBYTES + kb * KV_VSTR + vh * 352 + vj * 2;
      const int c0 = 16 * wm + 4 * g;
      if (c0 < 20) {
        if (!isv) {
          u32x2 v = {pk2(acc[0], acc[1]), pk2(acc[2], acc[3])};
          *(u32x2*)(kvp + key * 48 + c0 * 2) = v;
        } else {
#pragma unroll
          for (int j = 0; j < 4; ++j) *(u16*)(vbase + (c0 + j) * 16) = f2bf(acc[j]);
        }
      } else if (c0 == 20) {
        if (!isv) {
          u32x2 v = {0x00003f80u, 0u};
          *(u32x2*)(kvp + key * 48 + 40) = v;
        } else {
          *(u16*)(vbase + 20 * 16) = 0x3f80;
          *(u16*)(vbase + 21 * 16) = 0;
        }
      }
    } else {
      u16* h = (u16*)(ws + WS_H);
      float* gate = (float*)(ws + WS_G);
      u32x4 xa[4], xb[4];
#pragma unroll
      for (int rr = 0; rr < 4; ++rr) {
        const int row = isv * 16 + (wave - 4) * 4 + rr;
        const int sw = (row >> 1) & 7;
        xa[rr] = *(const u32x4*)(ximg + (lane >> 3) * 4096 + row * 128 + (((lane & 7) ^ sw) << 4));
        xb[rr] = u32x4{0u, 0u, 0u, 0u};
        if (two) xb[rr] = *(const u32x4*)(ximg + (8 + (lane >> 3)) * 4096 + row * 128 + (((lane & 7) ^ sw) << 4));
      }
      float sum[4], dot[4], var[4];
#pragma unroll
      for (int rr = 0; rr < 4; ++rr) {
        float sm = 0.f, dt = 0.f;
#pragma unroll
        for (int i = 0; i < 4; ++i) {
          const float a0 = __uint_as_float(xa[rr][i] << 16), a1 = __uint_as_float(xa[rr][i] & 0xffff0000u);
          const float b0 = __uint_as_float(xb[rr][i] << 16), b1 = __uint_as_float(xb[rr][i] & 0xffff0000u);
          sm += (a0 + a1) + (b0 + b1);
          dt += a0 * __uint_as_float(g0[i] << 16) + a1 * __uint_as_float(g0[i] & 0xffff0000u);
          dt += b0 * __uint_as_float(g1[i] << 16) + b1 * __uint_as_float(g1[i] & 0xffff0000u);
        }
        sum[rr] = sm; dot[rr] = dt;
      }
#pragma unroll
      for (int rr = 0; rr < 4; ++rr) { sum[rr] = wave_sum(sum[rr]); dot[rr] = wave_sum(dot[rr]); }
#pragma unroll
      for (int rr = 0; rr < 4; ++rr) {
        const float mean = sum[rr] * (1.f / DM);
        sum[rr] = mean;
        float vr = 0.f;
#pragma unroll
        for (int i = 0; i < 4; ++i) {
          const float a0 = __uint_as_float(xa[rr][i] << 16) - mean, a1 = __uint_as_float(xa[rr][i] & 0xffff0000u) - mean;
          const float b0 = __uint_as_float(xb[rr][i] << 16) - mean, b1 = __uint_as_float(xb[rr][i] & 0xffff0000u) - mean;
          vr += a0 * a0 + a1 * a1;
          if (two) vr += b0 * b0 + b1 * b1;
        }
        var[rr] = vr;
      }
#pragma unroll
      for (int rr = 0; rr < 4; ++rr) var[rr] = wave_sum(var[rr]);
#pragma unroll
      for (int rr = 0; rr < 4; ++rr) {
        const int s = s0 + isv * 16 + (wave - 4) * 4 + rr;
        const float mean = sum[rr];
        const float rstd = 1.f / sqrtf(var[rr] * (1.f / DM) + 1e-5f);
        u32x4 o0, o1;
#pragma unroll
        for (int i = 0; i < 4; ++i) {
          o0[i] = pk2((__uint_as_float(xa[rr][i] << 16) - mean) * rstd, (__uint_as_float(xa[rr][i] & 0xffff0000u) - mean) * rstd);
          o1[i] = pk2((__uint_as_float(xb[rr][i] << 16) - mean) * rstd, (__uint_as_float(xb[rr][i] & 0xffff0000u) - mean) * rstd);
        }
        *(u32x4*)(h + (size_t)s * DM + lane * 8) = o0;
        if (two) *(u32x4*)(h + (size_t)s * DM + 512 + lane * 8) = o1;
        if (lane == 0) gate[s] = 1.f / (1.f + __expf(-dot[rr]));
      }
    }
  }
}

constexpr int GM_WGS = 256;
constexpr int AT_WGS = SEQ / 8;
constexpr int G_NST = 4;
constexpr int AT_STG = 2 * KV_CHBYTES;
constexpr int S2_FLAG = 3 * AT_STG;
constexpr int S2_SHIFT = S2_FLAG + 64;
constexpr int S2_LDS = S2_SHIFT + 4096;

template <bool SAFE>
DI void attn_pass(const char* __restrict__ ws, char* lds, int srow, f32x16& o, int wave, int lane) {
  const int hd = lane & 31, hh = lane >> 5;
  const int half = wave >> 3;
  const char* kvg = ws + WS_KV;
  bf16x8 qf0;
  bf16x4 qf1;
  {
    const u16* q = (const u16*)(ws + WS_Q) + (size_t)srow * DM + hd * DH;
    u32x2 a = *(const u32x2*)(q + 8 * hh), b = *(const u32x2*)(q + 8 * hh + 4), c = *(const u32x2*)(q + 16);
    u32x4 t0 = {a[0], a[1], b[0], b[1]};
    u32x2 t1 = {hh == 0 ? c[0] : 0u, hh == 0 ? c[1] : 0u};
    qf0 = __builtin_bit_cast(bf16x8, t0);
    qf1 = __builtin_bit_cast(bf16x4, t1);
  }
  const int koff0 = half * KV_CHBYTES + hd * 48 + 16 * hh, koff1 = half * KV_CHBYTES + hd * 48 + 32 + 8 * hh;
  const int voff = half * KV_CHBYTES + KV_KBYTES + hh * 352 + (hd < 21 ? hd : 21) * 16;
  const int pc0 = wave, pc1 = 16 + wave;
  const char* gsrc0 = kvg + (size_t)((pc0 / 12) * (NCH / 2)) * KV_CHBYTES + (pc0 % 12) * 1024 + lane * 16;
  const int gd1 = (NCH / 2) * KV_CHBYTES + (pc1 - 12) * 1024 - pc0 * 1024;
  const unsigned ldst = lds_addr(lds);
  auto stage = [&](int boff) {
    glds16(gsrc0, ldst + boff + pc0 * 1024);
    if (wave < 8) glds16(gsrc0 + gd1, ldst + boff + pc1 * 1024);
    gsrc0 += KV_CHBYTES;
  };
  float mrun = -3.0e38f;
  stage(0);
  stage(AT_STG);
  if (wave < 8) WAITV(2); else WAITV(1);
  BARRIER();
  int bcur = 0;
  bf16x8 k0 = *(const bf16x8*)(lds + koff0);
  bf16x4 k1 = *(const bf16x4*)(lds + koff1);
  bf16x8 v0 = *(const bf16x8*)(lds + voff);
  bf16x8 v1 = *(const bf16x8*)(lds + voff + KV_VSTR);
  if (!SAFE) {
    f32x16 z = {};
    f32x16 s = MFMA32(k0, qf0, z);
    s = MFMA32K8(k1, qf1, s);
    float m = s[0];
#pragma unroll
    for (int i = 1; i < 16; ++i) m = fmaxf(m, s[i]);
    m = max_halves(m);
    const u16 mb = f2bf(m);
    *(float*)(lds + S2_SHIFT + (wave * 64 + lane) * 4) = bf2f(mb);
    if (hh == 1) qf1[0] = (short)(mb ^ 0x8000u);
  }
#pragma unroll 1
  for (int ch = 0; ch < NCH / 2; ++ch) {
    const char* base = lds + bcur;
    const int bnext = bcur == 2 * AT_STG ? 0 : bcur + AT_STG;
#pragma unroll SAFE ? 1 : 4
    for (int t = 0; t < KVCH / 32; ++t) {
      f32x16 z = {};
      f32x16 s = MFMA32(k0, qf0, z);
      s = MFMA32K8(k1, qf1, s);
      bf16x8 cv0 = v0, cv1 = v1;
      if (t + 1 < KVCH / 32) {
        k0 = *(const bf16x8*)(base + (t + 1) * 32 * 48 + koff0);
        k1 = *(const bf16x4*)(base + (t + 1) * 32 * 48 + koff1);
        v0 = *(const bf16x8*)(base + voff + (2 * t + 2) * KV_VSTR);
        v1 = *(const bf16x8*)(base + voff + (2 * t + 3) * KV_VSTR);
      } else {
        WAITV(0);
        BARRIER();
        stage(bnext == 2 * AT_STG ? 0 : bnext + AT_STG);
        const char* nb = lds + bnext;
        k0 = *(const bf16x8*)(nb + koff0);
        k1 = *(const bf16x4*)(nb + koff1);
        v0 = *(const bf16x8*)(nb + voff);
        v1 = *(const bf16x8*)(nb + voff + KV_VSTR);
      }
      if (SAFE) {
        float m = s[0];
#pragma unroll
        for (int i = 1; i < 16; ++i) m = fmaxf(m, s[i]);
        m = max_halves(m);
        const float mn = fmaxf(mrun, m);
        const float alpha = __builtin_amdgcn_exp2f(mrun - mn);
        mrun = mn;
#pragma unroll
        for (int i = 0; i < 16; ++i) { o[i] *= alpha; s[i] -= mn; }
      }
      u32x4 p0, p1;
#pragma unroll
      for (int i = 0; i < 4; ++i) {
        p0[i] = pk2(__builtin_amdgcn_exp2f(s[2 * i]), __builtin_amdgcn_exp2f(s[2 * i + 1]));
        p1[i] = pk2(__builtin_amdgcn_exp2f(s[8 + 2 * i]), __builtin_amdgcn_exp2f(s[8 + 2 * i + 1]));
      }
      o = MFMA32(cv0, __builtin_bit_cast(bf16x8, p0), o);
      o = MFMA32(cv1, __builtin_bit_cast(bf16x8, p1), o);
    }
    bcur = bnext;
  }
  WAITV(0);
  if (SAFE) *(float*)(lds + S2_SHIFT + (wave * 64 + lane) * 4) = mrun;
}

__global__ __launch_bounds__(1024, 6) void stage2_kernel(const u16* __restrict__ w1, const u16* __restrict__ b1,
                                                         const u16* __restrict__ wo, const u16* __restrict__ w2,
                                                         const u16* __restrict__ b2, char* __restrict__ ws,
                                                         u16* __restrict__ out) {
  extern __shared__ __attribute__((aligned(16))) char lds[];
  const int tid = threadIdx.x, lane = tid & 63;
  const int wave = __builtin_amdgcn_readfirstlane(tid >> 6);
  if (blockIdx.x < GM_WGS) {
    if (wave >= 8) return;
    const int bid = blockIdx.x;
    const int xcd = bid & 7, j = bid >> 3;
    const char* cnt = ws + WS_CNT;
    u16* u = (u16*)(ws + WS_U);
    const int ts = xcd * 4 + (j >> 3), td = j & 7;
    const int d0 = td * 80, s0 = ts * 64;
    if (wave >= 4) {
      if (j < 30) {
        const int tf = xcd * 3 + j / 10, t1 = j % 10;
        GemmSeg sg;
        seg_init<false>(sg, (const u16*)(ws + WS_H) + t1 * 64, DM, w1 + tf * 64, FF, SEQ / 64, (t1 * 3 + (j / 10)) & 31,
                        tid - 256);
        gemm_loader<4, G_NST, 0, 0>(sg, SEQ / 64, sg, 0, lds, wave - 4);
        __builtin_amdgcn_s_barrier();
      }
      wait_count(cnt + (CNT_U + d0 / 64) * 128, 24);
      wait_count(cnt + (CNT_U + (d0 + 79) / 64) * 128, 24);
      GemmSeg sb, sa;
      seg_init<false>(sb, u + d0, DM, w2 + s0, SEQ, FF / 64, td * 3, tid - 256);
      seg_init<true>(sa, wo + d0, DM, (const u16*)(ws + WS_O) + (size_t)s0 * DM, DM, DM / 64, (td + 3 * (j >> 3)) % 10,
                     tid - 256);
      gemm_loader<5, G_NST, 1, 2>(sb, FF / 64, sa, DM / 64, lds, wave - 4, cnt + (CNT_O + ts) * 128, 8);
      return;
    }
    const FragAddr f = frag_addr(wave, lane);
    if (j < 30) {
      const int tf = xcd * 3 + j / 10, t1 = j % 10;
      const int fcol = tf * 64 + 16 * wave + (lane & 15);
      const float bias = bf2f(b1[fcol]);
      f32x4 acc[5] = {};
      int buf = 0;
      gemm_consume<false, 4, G_NST>(acc, SEQ / 64, buf, lds, f);
#pragma unroll
      for (int ct = 0; ct < 4; ++ct) {
        float r[4];
#pragma unroll
        for (int i = 0; i < 4; ++i) {
          const float t = acc[ct][i] + bias;
          const float z = 0.7978845608028654f * (t + 0.044715f * t * t * t);
          r[i] = t / (1.f + __expf(-2.f * z));
        }
        u32x2 v = {pk2(r[0], r[1]), pk2(r[2], r[3])};
        store8_dev(u + (size_t)fcol * DM + t1 * 64 + 16 * ct + 4 * (lane >> 4), v);
      }
      WAITV(0);
      BARRIER();
      if (tid == 0)
        __hip_atomic_fetch_add((gu32*)(cnt + (CNT_U + t1) * 128), 1u, __ATOMIC_RELAXED, __HIP_MEMORY_SCOPE_AGENT);
    }
    const int s = s0 + 16 * wave + (lane & 15);
    const float g = ((const float*)(ws + WS_G))[s], bias = bf2f(b2[s]);
    f32x4 acc[5] = {};
    {
      int buf = 0;
      gemm_consume<false, 5, G_NST>(acc, FF / 64, buf, lds, f);
#pragma unroll
      for (int ct = 0; ct < 5; ++ct)
#pragma unroll
        for (int i = 0; i < 4; ++i) acc[ct][i] = (acc[ct][i] + bias) * (1.f - g);
      gemm_consume<true, 5, G_NST>(acc, DM / 64, buf, lds, f);
    }
#pragma unroll
    for (int ct = 0; ct < 5; ++ct) {
      u32x2 v = {pk2(acc[ct][0], acc[ct][1]), pk2(acc[ct][2], acc[ct][3])};
      *(u32x2*)(out + (size_t)s * DM + d0 + 16 * ct + 4 * (lane >> 4)) = v;
    }
    return;
  }
  const int half = wave >> 3;
  const int s = (blockIdx.x - GM_WGS) * 8 + (wave & 7);
  f32x16 o = {};
  volatile unsigned* flag = (volatile unsigned*)(lds + S2_FLAG);
  if (tid == 0) *flag = 0u;
  attn_pass<false>(ws, lds, s, o, wave, lane);

  {
    const float l = upper_half(o[8]);
    const bool bad = !(l > 8.67e-19f && l < 1.15e18f);
    if (bad) *flag = 1u;
    __syncthreads();
    if (__builtin_amdgcn_readfirstlane(*flag) != 0u) {
      o = f32x16{};
      attn_pass<true>(ws, lds, s, o, wave, fresh_lane());
    }
  }
  const int lane2 = fresh_lane();
  const int hd = lane2 & 31, hh = lane2 >> 5;
  __syncthreads();
  float* mg = (float*)lds + (wave & 7) * 13 * 64;
  if (half == 1) {
#pragma unroll
    for (int i = 0; i < 12; ++i) mg[i * 64 + lane2] = o[i];
  }
  __syncthreads();
  if (half == 0) {
    const float shift = *(const float*)(lds + S2_SHIFT + (wave * 64 + lane2) * 4);
    const float sh1 = *(const float*)(lds + S2_SHIFT + ((wave + 8) * 64 + lane2) * 4);
    const float mm = fmaxf(shift, sh1);
    const float f0 = __builtin_amdgcn_exp2f(shift - mm), f1 = __builtin_amdgcn_exp2f(sh1 - mm);
#pragma unroll
    for (int i = 0; i < 12; ++i) o[i] = o[i] * f0 + mg[i * 64 + lane2] * f1;
    const float l = upper_half(o[8]);
    const float inv = ((const float*)(ws + WS_G))[s] / l;
    u16* op = (u16*)(ws + WS_O) + (size_t)s * DM + hd * DH + 4 * hh;
#pragma unroll
    for (int g = 0; g < 3; ++g) {
      if (g < 2 || hh == 0) {
        u32x2 v = {pk2(o[4 * g] * inv, o[4 * g + 1] * inv), pk2(o[4 * g + 2] * inv, o[4 * g + 3] * inv)};
        store8_dev(op + 8 * g, v);
      }
    }
    WAITV(0);
  }
  __syncthreads();
  if (wave == 0 && lane2 == 0)
    __hip_atomic_fetch_add((gu32*)(ws + WS_CNT + (CNT_O + (s >> 6)) * 128), 1u, __ATOMIC_RELAXED,
                           __HIP_MEMORY_SCOPE_AGENT);
}

extern "C" void kernel_launch(void* const* d_in, const int* in_sizes, int n_in, void* d_out, int out_size, void* d_ws,
                              size_t ws_size, hipStream_t stream) {
  const u16* x = (const u16*)d_in[0];
  const u16* wq = (const u16*)d_in[1];
  const u16* wk = (const u16*)d_in[2];
  const u16* wv = (const u16*)d_in[3];
  const u16* wo = (const u16*)d_in[4];
  const u16* w1 = (const u16*)d_in[6];
  const u16* b1 = (const u16*)d_in[7];
  const u16* w2 = (const u16*)d_in[8];
  const u16* b2 = (const u16*)d_in[9];
  const u16* gw = (const u16*)d_in[10];
  char* ws = (char*)d_ws;
  (void)hipFuncSetAttribute((const void*)stage1_kernel, hipFuncAttributeMaxDynamicSharedMemorySize, S1_LDS);
  (void)hipFuncSetAttribute((const void*)stage2_kernel, hipFuncAttributeMaxDynamicSharedMemorySize, S2_LDS);
  stage1_kernel<<<S1_QT + S1_KVT, 512, S1_LDS, stream>>>(x, wq, wk, wv, gw, ws);
  stage2_kernel<<<GM_WGS + AT_WGS, 1024, S2_LDS, stream>>>(w1, b1, wo, w2, b2, ws, (u16*)d_out);
}
```
